```python
import torch

def _softmax(x, axis=-1):
    return torch.softmax(x, dim=axis)
def _gelu(x):
    return torch.nn.functional.gelu(x, approximate="tanh")
def _silu(x):
    return torch.nn.functional.silu(x)
def _sigmoid(x):
    return torch.sigmoid(x)
def _layernorm(x, axis=-1, eps=1e-5):
    m = x.mean(dim=axis, keepdim=True)
    v = x.var(dim=axis, keepdim=True, unbiased=False)
    return (x - m) / torch.sqrt(v + eps)
def _scatter_add(tgt, idx, src, axis=0):
    return tgt.clone().index_add_(axis, idx, src)
def _segment_sum(src, seg, n):
    out = torch.zeros((n,) + src.shape[1:], dtype=src.dtype)
    return out.index_add_(0, seg, src)
def _pad(x, pw):
    flat = tuple(v for pair in reversed(pw) for v in pair)
    return torch.nn.functional.pad(x, flat)

kernel_name = 'sequential_preLN__grouped_kv_mqa__dense_activation_mish__prompt'

def setup_inputs():

    _w = (lambda a: (lambda t: t.to(torch.bfloat16) if t.is_floating_point() else t)(torch.as_tensor(a)))
    def _A_setup():
        B, S, D, H = 1, 2048, 512, 32
        Dh = D // H
        return {
            "x": _w((lambda *s: torch.randn(*s, dtype=torch.bfloat16) * 0.05)(B, S, D)),
            "wq": _w((lambda *s: torch.randn(*s, dtype=torch.bfloat16) * 0.05)(D, H * Dh)),
            "wk": _w((lambda *s: torch.randn(*s, dtype=torch.bfloat16) * 0.05)(D, Dh)),
            "wv": _w((lambda *s: torch.randn(*s, dtype=torch.bfloat16) * 0.05)(D, Dh)),
            "wo": _w((lambda *s: torch.randn(*s, dtype=torch.bfloat16) * 0.05)(H * Dh, D)),
        }
    _A_in = _A_setup()

    def _B_setup():
        B, S, D, F = 1, 2048, 512, 1792
        return {
            "x": _w((lambda *s: torch.randn(*s, dtype=torch.bfloat16) * 0.05)(B, S, D)),
            "w1": _w((lambda *s: torch.randn(*s, dtype=torch.bfloat16) * 0.8)(D, F) * (D ** -0.5)),
            "b1": _w((lambda *s: torch.randn(*s, dtype=torch.bfloat16) * 0.4)(F)),
            "w2": _w((lambda *s: torch.randn(*s, dtype=torch.bfloat16) * 0.2)(F, D) * (F ** -0.5)),
        }
    _B_in = _B_setup()

    return {
        "A_x": _A_in["x"],
        "A_wq": _A_in["wq"],
        "A_wk": _A_in["wk"],
        "A_wv": _A_in["wv"],
        "A_wo": _A_in["wo"],
        "B_x": _B_in["x"],
        "B_w1": _B_in["w1"],
        "B_b1": _B_in["b1"],
        "B_w2": _B_in["w2"],
    }

def reference(A_x, A_wq, A_wk, A_wv, A_wo, B_x, B_w1, B_b1, B_w2):
    def _A_ref(x, wq, wk, wv, wo):
        B, S, D = x.shape
        H = 32
        Dh = D // H
        q = (lambda x, *d: x.permute(*d))((x @ wq).reshape(B, S, H, Dh), 0, 2, 1, 3)
        k = x @ wk
        v = x @ wv
        scores = torch.einsum("bhqd,bkd->bhqk", q, k) * (Dh ** -0.5)
        p = _softmax(scores, axis=-1)
        o = torch.einsum("bhqk,bkd->bhqd", p, v)
        return (lambda x, *d: x.permute(*d))(o, 0, 2, 1, 3).reshape(B, S, H * Dh) @ wo

    def _B_ref(x, w1, b1, w2):
        h = torch.einsum("bsd,df->bsf", x, w1) + b1
        sp = torch.log1p(torch.exp(-torch.abs(h))) + torch.where(h > 0, h, torch.zeros_like(h))
        act = h * torch.tanh(sp)
        return torch.einsum("bsf,fd->bsd", act, w2)

    x = A_x
    h = x + _A_ref(_layernorm(x, axis=-1), A_wq, A_wk, A_wv, A_wo)
    h2 = h + _B_ref(_layernorm(h, axis=-1), B_w1, B_b1, B_w2)
    return h2
```

```cpp
#include <hip/hip_runtime.h>
#include <hip/hip_bf16.h>

typedef __attribute__((ext_vector_type(8))) __bf16 bf16x8;
typedef __attribute__((ext_vector_type(4))) __bf16 bf16x4;
typedef __attribute__((ext_vector_type(2))) __bf16 bf16x2;
typedef __attribute__((ext_vector_type(16))) float f32x16;
typedef __attribute__((ext_vector_type(4))) float f32x4;
typedef __attribute__((ext_vector_type(2))) float f32x2;
typedef __attribute__((ext_vector_type(4))) unsigned u32x4;
typedef __attribute__((ext_vector_type(2))) unsigned u32x2;
typedef __attribute__((ext_vector_type(4))) short s16x4;
typedef __amdgpu_buffer_rsrc_t rsrc_t;
#define LDS3 __attribute__((address_space(3)))
#define GLB1 __attribute__((address_space(1)))
#define DEVI __device__ __forceinline__
#define SB __builtin_amdgcn_sched_barrier(0)
#define B8(x) __builtin_bit_cast(bf16x8, x)
#define MF32(a, b, c) __builtin_amdgcn_mfma_f32_32x32x16_bf16(a, b, c, 0, 0, 0)
#define MF16(a, b, c) __builtin_amdgcn_mfma_f32_16x16x32_bf16(a, b, c, 0, 0, 0)
#define STAMP(i)
#define STAMP1(i)

constexpr unsigned FLAG_OFF = 0, KMAX_OFF = 16384, KV_OFF = 32768, Q_OFF = 0x40000, WO_OFF = Q_OFF + (2u << 20),
                   W1_OFF = WO_OFF + (512u << 10), W2_OFF = W1_OFF + (1792u << 10), O_OFF = W2_OFF + (1792u << 10),
                   H_OFF = O_OFF + (2u << 20), P_OFF = H_OFF + (2u << 20), WS_END = P_OFF + (16u << 20);
constexpr float LOG2E = 1.4426950408889634f;

DEVI unsigned cvtpk(float a, float b) { f32x2 v = {a, b}; return __builtin_bit_cast(unsigned, __builtin_convertvector(v, bf16x2)); }
DEVI float blo(unsigned u) { return __builtin_bit_cast(float, u << 16); }
DEVI float bhi(unsigned u) { return __builtin_bit_cast(float, u & 0xffff0000u); }
DEVI rsrc_t mkrsrc(const void* p, unsigned bytes) { return __builtin_amdgcn_make_buffer_rsrc((void*)p, 0, bytes, 0x00020000); }
DEVI void dma16(rsrc_t rs, unsigned voff, unsigned soff, unsigned lds) {
    unsigned keep;
    asm volatile("s_mov_b32 %0, m0\n\ts_mov_b32 m0, %1\n\ts_nop 4\n\tbuffer_load_dwordx4 %2, %3, %4 offen lds\n\ts_mov_b32 m0, %0"
                 : "=&s"(keep) : "s"(lds), "v"(voff), "s"(rs), "s"(soff) : "memory");
}
#define WAITV(n) asm volatile("s_waitcnt vmcnt(" #n ")" ::: "memory")
#define WAITL() asm volatile("s_waitcnt lgkmcnt(0)" ::: "memory")
#define BAR() asm volatile("s_barrier" ::: "memory")
DEVI u32x4 lds128(unsigned a) { return *(LDS3 u32x4*)(size_t)a; }
DEVI void sts128(unsigned a, u32x4 v) { *(LDS3 u32x4*)(size_t)a = v; }
DEVI f32x2 swp32(float x) { float y = x; asm("s_nop 1\n\tv_permlane32_swap_b32 %0, %1\n\ts_nop 1" : "+v"(x), "+v"(y)); return f32x2{x, y}; }
DEVI f32x2 swp16(float x) { float y = x; asm("s_nop 1\n\tv_permlane16_swap_b32 %0, %1\n\ts_nop 1" : "+v"(x), "+v"(y)); return f32x2{x, y}; }
DEVI float xsum32(float x) { const f32x2 t = swp32(x); return t[0] + t[1]; }
DEVI float xmax32(float x) { const f32x2 t = swp32(x); return fmaxf(t[0], t[1]); }
DEVI float xsum16(float x) { const f32x2 t = swp16(x); return t[0] + t[1]; }
DEVI float xmax16(float x) { const f32x2 t = swp16(x); return fmaxf(t[0], t[1]); }
template <int C> DEVI float dppf(float x) { return __builtin_bit_cast(float, __builtin_amdgcn_update_dpp(0, __builtin_bit_cast(int, x), C, 0xf, 0xf, true)); }
DEVI float rowsum16(float x) { x += dppf<0xB1>(x); x += dppf<0x4E>(x); x += dppf<0x141>(x); x += dppf<0x140>(x); return x; }
DEVI float rowmax16(float x) { x = fmaxf(x, dppf<0xB1>(x)); x = fmaxf(x, dppf<0x4E>(x)); x = fmaxf(x, dppf<0x141>(x)); x = fmaxf(x, dppf<0x140>(x)); return x; }
DEVI float wavesum(float x) { return xsum32(xsum16(rowsum16(x))); }
DEVI float wavemax(float x) { return xmax32(xmax16(rowmax16(x))); }
DEVI float dot2(unsigned a, unsigned b, float c) { return __builtin_amdgcn_fdot2_f32_bf16(__builtin_bit_cast(bf16x2, a), __builtin_bit_cast(bf16x2, b), c, false); }

constexpr unsigned L1_X = 0, L1_WQ = 65536, L1_WKV = 131072, L1_END = 147456;
__global__ __launch_bounds__(512, 2) void k1_kernel(const unsigned short* __restrict__ x, const unsigned short* __restrict__ wq,
        const unsigned short* __restrict__ wk, const unsigned short* __restrict__ wv, const unsigned short* __restrict__ wo,
        const unsigned short* __restrict__ w1, const unsigned short* __restrict__ w2, char* __restrict__ ws) {
    __shared__ __align__(16) char smem[L1_END];
    const unsigned sb = (unsigned)(size_t)(LDS3 char*)smem;
    const int tid = threadIdx.x, lane = tid & 63, w = __builtin_amdgcn_readfirstlane(tid >> 6);
    const int b = blockIdx.x, g = (b & 7) * 4 + (b >> 6), j = (b >> 3) & 7;
    const rsrc_t rx = mkrsrc(x, 2048 * 512 * 2), rq = mkrsrc(wq, 512 * 512 * 2), rkv = mkrsrc((j & 1) ? wv : wk, 512 * 16 * 2);
    STAMP1(0);
    u32x4 xr[8];
#pragma unroll
    for (int i = 0; i < 8; i++) xr[i] = __builtin_amdgcn_raw_buffer_load_b128(rx, lane * 16, (g * 64 + w * 8 + i) * 1024, 0);
    {
        const int rl = lane >> 3;
#pragma unroll
        for (int i = 0; i < 8; i++) {
            const int row = 64 * w + 8 * i + rl, f = ((rl >> 1) & 1) | ((i & 1) << 1);
            dma16(rq, row * 1024 + j * 128 + ((((lane & 7) >> 1) ^ f) * 32) + (lane & 1) * 16, 0, sb + L1_WQ + (w * 8 + i) * 1024);
        }
#pragma unroll
        for (int i = 0; i < 2; i++) dma16(rkv, lane * 16, (w * 2 + i) * 1024, sb + L1_WKV + (w * 2 + i) * 1024);
    }
    unsigned short pk[2][8];
    unsigned pdst[2];
#pragma unroll
    for (int u = 0; u < 2; u++) {
        const int id = (b * 8 + w) + u * 2048;
        const unsigned short* src; int ld, k0, n0;
        if (id < 512)       { src = wo; ld = 512;  k0 = (id & 31) * 16; n0 = (id >> 5) * 32; }
        else if (id < 2304) { const int i2 = id - 512; src = w1; ld = 1792; k0 = (i2 & 31) * 16; n0 = (i2 >> 5) * 32; }
        else                { const int i2 = id - 2304, jj = i2 / 224, rem = i2 - jj * 224, ct = rem / 14, ks = rem - ct * 14;
                              src = w2; ld = 512; k0 = jj * 224 + ks * 16; n0 = ct * 32; }
        const unsigned short* p = src + (size_t)(k0 + 4 * (lane >> 5)) * ld + n0 + (lane & 31);
#pragma unroll
        for (int e = 0; e < 8; e++) pk[u][e] = p[(8 * (e >> 2) + (e & 3)) * ld];
        pdst[u] = WO_OFF + id * 1024;
    }
    if (j == 0 && tid < 128) ((unsigned*)(ws + FLAG_OFF))[g * 128 + tid] = 0u;
#pragma unroll
    for (int i = 0; i < 8; i++) {
        float s = 0.f, q = 0.f;
#pragma unroll
        for (int e = 0; e < 4; e++) { s = dot2(xr[i][e], 0x3f803f80u, s); q = dot2(xr[i][e], xr[i][e], q); }
        s = wavesum(s) * (1.f / 512.f); q = wavesum(q) * (1.f / 512.f);
        const float rstd = __builtin_amdgcn_rsqf(fmaxf(q - s * s, 0.f) + 1e-5f), nm = -s * rstd;
        u32x4 o;
#pragma unroll
        for (int e = 0; e < 4; e++) o[e] = cvtpk(fmaf(blo(xr[i][e]), rstd, nm), fmaf(bhi(xr[i][e]), rstd, nm));
        const int row = w * 8 + i;
        sts128(sb + L1_X + row * 1024 + ((lane ^ (row & 15)) * 16), o);
    }
#pragma unroll
    for (int u = 0; u < 2; u++) {
        u32x4 o;
#pragma unroll
        for (int e = 0; e < 4; e++) o[e] = (unsigned)pk[u][2 * e] | ((unsigned)pk[u][2 * e + 1] << 16);
        *(u32x4*)(ws + pdst[u] + lane * 16) = o;
    }
    STAMP1(1);
    WAITV(2);
    WAITL(); BAR();
    STAMP1(2);
    const int c = lane >> 4, li = lane & 15, hq = w & 3, tt = w >> 2;
    const unsigned arow = 8 * c + (li >> 2);
    const unsigned abase = sb + L1_WQ + arow * 128 + ((hq ^ (((li >> 3) & 1) | ((c & 1) << 1))) * 32) + (li & 3) * 8;
    unsigned bb[4];
#pragma unroll
    for (int m = 0; m < 4; m++) bb[m] = sb + L1_X + li * 1024 + (((4 * m + c) ^ li) * 16);
    f32x4 a0 = {0, 0, 0, 0}, a1 = {0, 0, 0, 0};
#pragma unroll
    for (int ks = 0; ks < 16; ks++) {
        const s16x4 lo = __builtin_amdgcn_ds_read_tr16_b64_v4i16((LDS3 s16x4*)(size_t)(abase + ks * 4096));
        const s16x4 hi = __builtin_amdgcn_ds_read_tr16_b64_v4i16((LDS3 s16x4*)(size_t)(abase + ks * 4096 + 512));
        const bf16x8 af = B8(__builtin_shufflevector(lo, hi, 0, 1, 2, 3, 4, 5, 6, 7));
        const unsigned ba = bb[ks & 3] + tt * 32768 + (ks >> 2) * 256;
        a0 = MF16(af, B8(lds128(ba)), a0);
        a1 = MF16(af, B8(lds128(ba + 16384)), a1);
    }
    {
        const float sc = 0.25f * LOG2E;
        char* qd = ws + Q_OFF + ((g * 2 + tt) * 32 + 4 * j + hq) * 1024 + ((c & 1) * 32 + li) * 16 + (c >> 1) * 8;
        *(u32x2*)qd = u32x2{cvtpk(a0[0] * sc, a0[1] * sc), cvtpk(a0[2] * sc, a0[3] * sc)};
        *(u32x2*)(qd + 256) = u32x2{cvtpk(a1[0] * sc, a1[1] * sc), cvtpk(a1[2] * sc, a1[3] * sc)};
    }
    if (w == 7) {
        const int sub = j >> 1, tile = g * 2 + (sub >> 1), s = sub & 1;
        const unsigned wb = sb + L1_WKV + arow * 32 + (li & 3) * 8;
        f32x4 d = {0, 0, 0, 0};
#pragma unroll
        for (int ks = 0; ks < 16; ks++) {
            const s16x4 lo = __builtin_amdgcn_ds_read_tr16_b64_v4i16((LDS3 s16x4*)(size_t)(wb + ks * 1024));
            const s16x4 hi = __builtin_amdgcn_ds_read_tr16_b64_v4i16((LDS3 s16x4*)(size_t)(wb + ks * 1024 + 128));
            const bf16x8 wf = B8(__builtin_shufflevector(lo, hi, 0, 1, 2, 3, 4, 5, 6, 7));
            const bf16x8 xf = B8(lds128(bb[ks & 3] + sub * 16384 + (ks >> 2) * 256));
            if (j & 1) d = MF16(xf, wf, d); else d = MF16(wf, xf, d);
        }
        const u32x2 o = {cvtpk(d[0], d[1]), cvtpk(d[2], d[3])};
        char* kv = ws + KV_OFF + tile * 2048;
        if (j & 1) *(u32x2*)(kv + 1024 + s * 512 + (c & 1) * 256 + li * 16 + (c >> 1) * 8) = o;
        else {
            *(u32x2*)(kv + ((c & 1) * 32 + 16 * s + li) * 16 + (c >> 1) * 8) = o;
            const float n2 = rowmax16(xsum32(xsum16(dot2(o[0], o[0], dot2(o[1], o[1], 0.f)))));
            if (lane == 0) ((float*)(ws + KMAX_OFF))[g * 4 + sub] = n2;
        }
    }
    STAMP1(3);
}

constexpr unsigned L2_ONES = 131072, L2_STAT = 135168, L2_X = 139264, L2_END = 163840;
DEVI void signal(char* ws, int g, int ph) {
    __hip_atomic_fetch_add((GLB1 unsigned*)(ws + FLAG_OFF) + g * 128 + ph * 32, 1u, __ATOMIC_RELAXED, __HIP_MEMORY_SCOPE_AGENT);
}
DEVI void await(char* ws, int g, int ph) {
    GLB1 unsigned* p = (GLB1 unsigned*)(ws + FLAG_OFF) + g * 128 + ph * 32;
    for (unsigned spins = 0; spins < (1u << 24); spins++) {
        if ((unsigned)__builtin_amdgcn_readfirstlane(__hip_atomic_load(p, __ATOMIC_RELAXED, __HIP_MEMORY_SCOPE_AGENT)) >= 8u) break;
        __builtin_amdgcn_s_sleep(1);
    }
    asm volatile("" ::: "memory");
}
#define LDW(voff, soff) __builtin_amdgcn_raw_buffer_load_b128(rw, voff, soff, 0)
#define LDC(voff, soff) __builtin_amdgcn_raw_buffer_load_b128(rw, voff, soff, 16)
#define STC(v, voff, soff) __builtin_amdgcn_raw_buffer_store_b128(v, rw, voff, soff, 16)
#define HANDOFF(ph) WAITV(0); BAR(); if (tid == 0) signal(ws, g, ph);

__global__ __launch_bounds__(512, 2) void k2_kernel(const unsigned short* __restrict__ x, const unsigned short* __restrict__ b1,
                                                    unsigned short* __restrict__ out, char* ws) {
    __shared__ __align__(16) char smem[L2_END];
    const unsigned sb = (unsigned)(size_t)(LDS3 char*)smem;
    const int tid = threadIdx.x, lane = tid & 63, w = __builtin_amdgcn_readfirstlane(tid >> 6);
    const int b = blockIdx.x, g = (b & 7) * 4 + (b >> 6), j = (b >> 3) & 7;
    const int r = lane & 31, h = lane >> 5, kh = w >> 2;
    const unsigned l16 = lane * 16;
    const rsrc_t rw = mkrsrc(ws, WS_END);
    STAMP(0);
    u32x4 q0, q1; u32x2 km;
    asm volatile("s_nop 4\n\tbuffer_load_dwordx4 %0, %3, %4, %5 offen\n\tbuffer_load_dwordx4 %1, %3, %4, %6 offen\n\tbuffer_load_dwordx2 %2, %7, %4, 0 offen"
                 : "=&v"(q0), "=&v"(q1), "=&v"(km)
                 : "v"(l16), "s"(rw), "s"(Q_OFF + ((g * 2) * 32 + 4 * j + (w & 3)) * 1024), "s"(Q_OFF + ((g * 2 + 1) * 32 + 4 * j + (w & 3)) * 1024),
                   "v"(KMAX_OFF + lane * 8) : "memory");
    const unsigned kvo = kh * 65536 + (w & 3) * 4096;
#define KVCHUNK(c) _Pragma("unroll") for (int i = 0; i < 4; i++) dma16(rw, l16, KV_OFF + kvo + (c) * 16384 + i * 1024, sb + kvo + (c) * 16384 + i * 1024);
#define WOPIECE(i) dma16(rw, l16, WO_OFF + (j * 64 + w * 8 + (i)) * 1024, sb + kvo + (w & 3) * 4096 + (i) * 1024);
    KVCHUNK(0) KVCHUNK(1)
    if (tid < 256) sts128(sb + L2_ONES + tid * 16, u32x4{0x3f803f80u, 0x3f803f80u, 0x3f803f80u, 0x3f803f80u});
    asm volatile("s_waitcnt vmcnt(8)" : "+v"(q0), "+v"(q1), "+v"(km) :: "memory");
    float qn0 = 0.f, qn1 = 0.f;
#pragma unroll
    for (int e = 0; e < 4; e++) { qn0 = dot2(q0[e], q0[e], qn0); qn1 = dot2(q1[e], q1[e], qn1); }
    const float kmax2 = wavemax(fmaxf(__builtin_bit_cast(float, km[0]), __builtin_bit_cast(float, km[1])));
    const float mb = __builtin_sqrtf(fmaxf(xsum32(qn0), xsum32(qn1)) * kmax2) * 1.001f;
    const bool fast = __all(mb <= 60.f);
    const unsigned w1o = W1_OFF + ((7 * j + (w < 7 ? w : 6)) * 32) * 1024;
    u32x4 w1f[32];
    f32x16 o0, o1;
    float m0, m1;
#pragma unroll
    for (int i = 0; i < 16; i++) { o0[i] = 0.f; o1[i] = 0.f; }
    unsigned ka = sb + kh * 65536 + l16, va = r < 16 ? sb + kh * 65536 + 1024 + h * 256 + r * 16 : sb + L2_ONES + ((r >= 20 && r < 28) ? 64 : 0);
    const unsigned vinc = r < 16 ? 2048 : 0;
    if (fast) {
        f32x16 nm, sA, sB;
#pragma unroll
        for (int i = 0; i < 16; i++) nm[i] = -mb;
        m0 = mb; m1 = mb;
        u32x4 p1 = {0, 0, 0, 0}, k0, k1, va0, va1, vb0, vb1;
#define EY(S, i) e[i] = __builtin_amdgcn_exp2f(S[i]); SB;
#define CV(P, k, i) P[k] = cvtpk(e[i], e[i + 1]); SB;
#define STEP(S_, OP, VP1, SN, KN, QN, OC, VC0, LD) { float e[16]; u32x4 p0;                                                   \
        EY(S_, 0) EY(S_, 1) OP = MF32(B8(VP1), B8(p1), OP); SB; EY(S_, 2) EY(S_, 3) CV(p0, 0, 0) EY(S_, 4) EY(S_, 5) CV(p0, 1, 2) EY(S_, 6) \
        SN = MF32(B8(KN), B8(QN), nm); SB; LD EY(S_, 7) CV(p0, 2, 4) EY(S_, 8) EY(S_, 9) CV(p0, 3, 6) EY(S_, 10) EY(S_, 11)       \
        OC = MF32(B8(VC0), B8(p0), OC); SB; CV(p1, 0, 8) EY(S_, 12) EY(S_, 13) CV(p1, 1, 10) EY(S_, 14) EY(S_, 15) CV(p1, 2, 12) CV(p1, 3, 14) }
#define LDKV(K_, V0_, V1_, T) K_ = lds128(ka + (T) * 2048); V0_ = lds128(va); V1_ = lds128(va + 512); va += vinc; asm volatile("" : "+v"(va)); SB;
        WAITV(4); WAITL(); BAR();
        LDKV(k0, va0, va1, 0)
        vb1 = va1;
        sA = MF32(B8(k0), B8(q0), nm);
#pragma unroll
        for (int it = 0; it < 16; it++) {
            if (it == 3) { WAITV(10); BAR(); }
            if (it == 7) { WAITV(14); BAR(); }
            if (it == 11) { WAITV(18); BAR(); }
            SB;
            STEP(sA, o1, vb1, sB, k0, q1, o0, va0, LDKV(k1, vb0, vb1, 2 * it + 1))
            if (it == 0) { KVCHUNK(2) } if (it == 1) { KVCHUNK(3) }
            if (it >= 2 && it < 14) { w1f[2 * it - 4] = LDW(l16, w1o + (2 * it - 4) * 1024); SB; }
            STEP(sB, o0, va1, sA, k1, q0, o1, va0, )
            if (it >= 11 && it < 15) { WOPIECE(2 * it - 22) WOPIECE(2 * it - 21) }
            STEP(sA, o1, va1, sB, k1, q1, o0, vb0, LDKV(k0, va0, va1, 2 * it + 2))
            if (it >= 2 && it < 14) { w1f[2 * it - 3] = LDW(l16, w1o + (2 * it - 3) * 1024); SB; }
            STEP(sB, o0, vb1, sA, k0, q0, o1, vb0, )
        }
        o1 = MF32(B8(vb1), B8(p1), o1);
    } else {
        KVCHUNK(2) KVCHUNK(3)
        WAITV(0); WAITL(); BAR();
        m0 = -3.0e38f; m1 = -3.0e38f;
        const f32x16 z = {0, 0, 0, 0, 0, 0, 0, 0, 0, 0, 0, 0, 0, 0, 0, 0};
#define SLOW(Q_, O_, M_) { f32x16 s = MF32(B8(kf), B8(Q_), z); float mx = s[0];                                               \
            _Pragma("unroll") for (int i = 1; i < 16; i++) mx = fmaxf(mx, s[i]);                                              \
            mx = fmaxf(xmax32(mx), M_); const float al = __builtin_amdgcn_exp2f(M_ - mx); M_ = mx; u32x4 p0, p1;              \
            _Pragma("unroll") for (int i = 0; i < 16; i++) O_[i] *= al;                                                       \
            _Pragma("unroll") for (int i = 0; i < 4; i++) {                                                                   \
                p0[i] = cvtpk(__builtin_amdgcn_exp2f(s[2 * i] - mx), __builtin_amdgcn_exp2f(s[2 * i + 1] - mx));              \
                p1[i] = cvtpk(__builtin_amdgcn_exp2f(s[2 * i + 8] - mx), __builtin_amdgcn_exp2f(s[2 * i + 9] - mx)); }        \
            O_ = MF32(B8(v0), B8(p0), O_); O_ = MF32(B8(v1), B8(p1), O_); }
#pragma unroll 1
        for (int t = 0; t < 32; t++) {
            if (t == 6 || t == 14 || t == 22) { WAITL(); BAR(); }
            if (t == 22) { _Pragma("unroll") for (int i = 0; i < 8; i++) WOPIECE(i) }
            const u32x4 kf = lds128(ka), v0 = lds128(va), v1 = lds128(va + 512);
            ka += 2048; va += vinc;
            SLOW(q0, o0, m0) SLOW(q1, o1, m1)
        }
#pragma unroll
        for (int i = 0; i < 24; i++) w1f[i] = LDW(l16, w1o + i * 1024);
    }
    STAMP(1);
    {
        const f32x16 os = kh ? o0 : o1, ok = kh ? o1 : o0;
        const float ms = kh ? m0 : m1, mk = kh ? m1 : m0;
        const unsigned xa = sb + L2_X + w * 2560, xb = sb + L2_X + (w ^ 4) * 2560;
        *(LDS3 f32x4*)(size_t)(xa + l16) = f32x4{os[0], os[1], os[2], os[3]};
        *(LDS3 f32x4*)(size_t)(xa + 1024 + l16) = f32x4{os[4], os[5], os[6], os[7]};
        *(LDS3 f32x2*)(size_t)(xa + 2048 + lane * 8) = f32x2{os[8], ms};
        WAITL(); BAR();
        const f32x4 a = *(LDS3 f32x4*)(size_t)(xb + l16), c = *(LDS3 f32x4*)(size_t)(xb + 1024 + l16);
        const f32x2 lm = *(LDS3 f32x2*)(size_t)(xb + 2048 + lane * 8);
        const float mm = fmaxf(mk, lm[1]), ea = __builtin_amdgcn_exp2f(mk - mm), eb = __builtin_amdgcn_exp2f(lm[1] - mm);
        const float inv = 1.f / (ok[8] * ea + lm[0] * eb), fa = ea * inv, fb = eb * inv;
        u32x4 of;
        of[0] = cvtpk(ok[0] * fa + a[0] * fb, ok[1] * fa + a[1] * fb); of[1] = cvtpk(ok[2] * fa + a[2] * fb, ok[3] * fa + a[3] * fb);
        of[2] = cvtpk(ok[4] * fa + c[0] * fb, ok[5] * fa + c[1] * fb); of[3] = cvtpk(ok[6] * fa + c[2] * fb, ok[7] * fa + c[3] * fb);
        STC(of, l16, O_OFF + ((g * 2 + kh) * 32 + 4 * j + (w & 3)) * 1024);
    }
    HANDOFF(0)
    STAMP(2);
    const int tt = w & 1, ct = (w >> 1) & 1;
    const unsigned xo = (64 * g + 32 * tt + r) * 512 + 64 * j + 32 * ct + 16 * kh;
    const u32x2 xa0 = *(const u32x2*)(x + xo + 4 * h), xa1 = *(const u32x2*)(x + xo + 8 + 4 * h);
    u32x2 bb[4];
#pragma unroll
    for (int i = 0; i < 4; i++) bb[i] = *(const u32x2*)(b1 + 224 * j + 32 * (w < 7 ? w : 6) + 8 * i + 4 * h);
    if (w == 0) await(ws, g, 0);
    BAR();
    STAMP(3);
    float hv[8];
    {
        u32x4 of[16];
#pragma unroll
        for (int i = 0; i < 16; i++) of[i] = LDC(l16, O_OFF + ((g * 2 + tt) * 32 + 16 * kh + i) * 1024);
        f32x16 acc = {0, 0, 0, 0, 0, 0, 0, 0, 0, 0, 0, 0, 0, 0, 0, 0};
        const unsigned wa = sb + ct * 65536 + kh * 16384 + l16;
        u32x4 af[4];
#pragma unroll
        for (int i = 0; i < 4; i++) af[i] = lds128(wa + i * 1024);
        SB;
#pragma unroll
        for (int i = 0; i < 16; i++) { acc = MF32(B8(af[i & 3]), B8(of[i]), acc); SB; if (i < 12) { af[i & 3] = lds128(wa + (i + 4) * 1024); SB; } }
        const unsigned xa = sb + L2_X + w * 2048 + l16, xb = sb + L2_X + (w ^ 4) * 2048 + l16;
        f32x4 s0, s1;
#pragma unroll
        for (int i = 0; i < 4; i++) { s0[i] = kh ? acc[i] : acc[8 + i]; s1[i] = kh ? acc[4 + i] : acc[12 + i]; }
        *(LDS3 f32x4*)(size_t)xa = s0; *(LDS3 f32x4*)(size_t)(xa + 1024) = s1;
        WAITL(); BAR();
        const f32x4 a = *(LDS3 f32x4*)(size_t)xb, c = *(LDS3 f32x4*)(size_t)(xb + 1024);
#pragma unroll
        for (int i = 0; i < 4; i++) { hv[i] = (kh ? acc[8 + i] : acc[i]) + a[i]; hv[4 + i] = (kh ? acc[12 + i] : acc[4 + i]) + c[i]; }
        hv[0] += blo(xa0[0]); hv[1] += bhi(xa0[0]); hv[2] += blo(xa0[1]); hv[3] += bhi(xa0[1]);
        hv[4] += blo(xa1[0]); hv[5] += bhi(xa1[0]); hv[6] += blo(xa1[1]); hv[7] += bhi(xa1[1]);
        const u32x4 hf = {cvtpk(hv[0], hv[1]), cvtpk(hv[2], hv[3]), cvtpk(hv[4], hv[5]), cvtpk(hv[6], hv[7])};
        STC(hf, l16, H_OFF + ((g * 2 + tt) * 32 + 4 * j + 2 * ct + kh) * 1024);
    }
    HANDOFF(1)
    STAMP(4);
#pragma unroll
    for (int i = 24; i < 32; i++) w1f[i] = LDW(l16, w1o + i * 1024);
    if (w == 0) await(ws, g, 1);
    BAR();
    STAMP(5);
    {
        u32x4 hf[2][4];
#pragma unroll
        for (int t = 0; t < 2; t++)
#pragma unroll
            for (int i = 0; i < 4; i++) hf[t][i] = LDC(l16, H_OFF + ((g * 2 + t) * 32 + 4 * w + i) * 1024);
        f32x2 st[2];
#pragma unroll
        for (int t = 0; t < 2; t++) {
            float s = 0.f, q = 0.f;
#pragma unroll
            for (int i = 0; i < 4; i++)
#pragma unroll
                for (int e = 0; e < 4; e++) { s = dot2(hf[t][i][e], 0x3f803f80u, s); q = dot2(hf[t][i][e], hf[t][i][e], q); }
            st[t] = f32x2{xsum32(s), xsum32(q)};
        }
        *(LDS3 f32x2*)(size_t)(sb + L2_STAT + w * 512 + lane * 8) = h ? st[1] : st[0];
        WAITL(); BAR();
#pragma unroll
        for (int t = 0; t < 2; t++) {
            f32x2 a = {0.f, 0.f};
#pragma unroll
            for (int i = 0; i < 8; i++) a += *(LDS3 f32x2*)(size_t)(sb + L2_STAT + i * 512 + t * 256 + r * 8);
            const float mean = a[0] * (1.f / 512.f), var = fmaxf(a[1] * (1.f / 512.f) - mean * mean, 0.f);
            const float rstd = __builtin_amdgcn_rsqf(var + 1e-5f), nm = -mean * rstd;
#pragma unroll
            for (int i = 0; i < 4; i++) {
                u32x4 o;
#pragma unroll
                for (int e = 0; e < 4; e++) o[e] = cvtpk(fmaf(blo(hf[t][i][e]), rstd, nm), fmaf(bhi(hf[t][i][e]), rstd, nm));
                sts128(sb + (t * 32 + 4 * w + i) * 1024 + l16, o);
            }
        }
        WAITL(); BAR();
    }
    STAMP(6);
    u32x4 w2f[28];
    const unsigned w2o = W2_OFF + (j * 224 + w * 28) * 1024;
    {
        f32x16 a0 = {0, 0, 0, 0, 0, 0, 0, 0, 0, 0, 0, 0, 0, 0, 0, 0}, a1 = a0;
        u32x4 b0[2], b1f[2];
#pragma unroll
        for (int i = 0; i < 2; i++) { b0[i] = lds128(sb + i * 1024 + l16); b1f[i] = lds128(sb + 32768 + i * 1024 + l16); }
        SB;
#pragma unroll
        for (int ks = 0; ks < 32; ks++) {
            a0 = MF32(B8(w1f[ks]), B8(b0[ks & 1]), a0); SB;
            if (ks < 30) { b0[ks & 1] = lds128(sb + (ks + 2) * 1024 + l16); SB; }
            a1 = MF32(B8(w1f[ks]), B8(b1f[ks & 1]), a1); SB;
            if (ks < 30) { b1f[ks & 1] = lds128(sb + 32768 + (ks + 2) * 1024 + l16); SB; }
            if (ks >= 2 && ks < 30) { w2f[ks - 2] = LDW(l16, w2o + (ks - 2) * 1024); SB; }
        }
#pragma unroll
        for (int t = 0; t < 2; t++) {
            float y[16];
#pragma unroll
            for (int i = 0; i < 16; i++) {
                const unsigned bw = bb[i >> 2][(i >> 1) & 1];
                const float z = (t ? a1[i] : a0[i]) + ((i & 1) ? bhi(bw) : blo(bw));
                const float e = __builtin_amdgcn_exp2f(fminf(z * LOG2E, 60.f)), n = e * (e + 2.f);
                y[i] = z * (n * __builtin_amdgcn_rcpf(n + 2.f));
            }
#pragma unroll
            for (int s = 0; s < 2; s++)
                if (w < 7) sts128(sb + 65536 + (t * 14 + 2 * w + s) * 1024 + l16,
                       u32x4{cvtpk(y[8 * s], y[8 * s + 1]), cvtpk(y[8 * s + 2], y[8 * s + 3]), cvtpk(y[8 * s + 4], y[8 * s + 5]), cvtpk(y[8 * s + 6], y[8 * s + 7])});
        }
    }
    WAITL(); BAR();
    STAMP(7);
    {
        f32x16 c[2][2];
#pragma unroll
        for (int i = 0; i < 16; i++) { c[0][0][i] = 0.f; c[0][1][i] = 0.f; c[1][0][i] = 0.f; c[1][1][i] = 0.f; }
#pragma unroll
        for (int cc = 0; cc < 2; cc++) {
#pragma unroll
            for (int ks = 0; ks < 14; ks++) {
                c[cc][0] = MF32(B8(w2f[cc * 14 + ks]), B8(lds128(sb + 65536 + ks * 1024 + l16)), c[cc][0]);
                c[cc][1] = MF32(B8(w2f[cc * 14 + ks]), B8(lds128(sb + 65536 + (14 + ks) * 1024 + l16)), c[cc][1]);
            }
#pragma unroll
            for (int t = 0; t < 2; t++)
#pragma unroll
                for (int s = 0; s < 2; s++) {
                    const f32x16 v = c[cc][t];
                    const u32x4 pf = {cvtpk(v[8 * s], v[8 * s + 1]), cvtpk(v[8 * s + 2], v[8 * s + 3]), cvtpk(v[8 * s + 4], v[8 * s + 5]), cvtpk(v[8 * s + 6], v[8 * s + 7])};
                    STC(pf, l16, P_OFF + (((g * 8 + w) * 8 + j) * 8 + t + 2 * cc + 4 * s) * 1024);
                }
        }
    }
    HANDOFF(2)
    STAMP(8);
    if (w == 0) await(ws, g, 2);
    BAR();
    STAMP(9);
    {
        u32x4 pf[8];
#pragma unroll
        for (int i = 0; i < 8; i++) pf[i] = LDC(l16, P_OFF + (((g * 8 + j) * 8 + i) * 8 + w) * 1024);
#pragma unroll
        for (int i = 0; i < 8; i++)
#pragma unroll
            for (int e = 0; e < 4; e++) { hv[2 * e] += blo(pf[i][e]); hv[2 * e + 1] += bhi(pf[i][e]); }
        const u32x2 s0 = __builtin_amdgcn_permlane32_swap(cvtpk(hv[0], hv[1]), cvtpk(hv[4], hv[5]), false, false);
        const u32x2 s1 = __builtin_amdgcn_permlane32_swap(cvtpk(hv[2], hv[3]), cvtpk(hv[6], hv[7]), false, false);
        *(u32x4*)(out + xo + 8 * h) = u32x4{s0[0], s1[0], s0[1], s1[1]};
    }
    STAMP(10);
}

extern "C" void kernel_launch(void* const* d_in, const int* in_sizes, int n_in, void* d_out, int out_size, void* d_ws, size_t ws_size,
                              hipStream_t stream) {
    typedef const unsigned short* P;
    k1_kernel<<<256, 512, 0, stream>>>((P)d_in[0], (P)d_in[1], (P)d_in[2], (P)d_in[3], (P)d_in[4], (P)d_in[6], (P)d_in[8], (char*)d_ws);
    k2_kernel<<<256, 512, 0, stream>>>((P)d_in[0], (P)d_in[7], (unsigned short*)d_out, (char*)d_ws);
}
```

```cpp
#include <hip/hip_runtime.h>
#include <hip/hip_bf16.h>

typedef __attribute__((ext_vector_type(8))) __bf16 bf16x8;
typedef __attribute__((ext_vector_type(4))) __bf16 bf16x4;
typedef __attribute__((ext_vector_type(2))) __bf16 bf16x2;
typedef __attribute__((ext_vector_type(16))) float f32x16;
typedef __attribute__((ext_vector_type(4))) float f32x4;
typedef __attribute__((ext_vector_type(2))) float f32x2;
typedef __attribute__((ext_vector_type(4))) unsigned u32x4;
typedef __attribute__((ext_vector_type(2))) unsigned u32x2;
typedef __attribute__((ext_vector_type(4))) short s16x4;
typedef __amdgpu_buffer_rsrc_t rsrc_t;
#define LDS3 __attribute__((address_space(3)))
#define GLB1 __attribute__((address_space(1)))
#define DEVI __device__ __forceinline__
#define SB __builtin_amdgcn_sched_barrier(0)
#define B8(x) __builtin_bit_cast(bf16x8, x)
#define MF32(a, b, c) __builtin_amdgcn_mfma_f32_32x32x16_bf16(a, b, c, 0, 0, 0)
#define MF16(a, b, c) __builtin_amdgcn_mfma_f32_16x16x32_bf16(a, b, c, 0, 0, 0)
#define STAMP(i)
#define STAMP1(i)
#define STAMP1DECL
#define STAMPDECL

constexpr unsigned FLAG_OFF = 0, KMAX_OFF = 16384, KV_OFF = 32768, Q_OFF = 0x40000, WO_OFF = Q_OFF + (2u << 20),
                   W1_OFF = WO_OFF + (512u << 10), W2_OFF = W1_OFF + (1792u << 10), O_OFF = W2_OFF + (1792u << 10),
                   H_OFF = O_OFF + (2u << 20), A_OFF = H_OFF + (2u << 20), WS_END = A_OFF + (7u << 20);
constexpr float LOG2E = 1.4426950408889634f;

DEVI unsigned cvtpk(float a, float b) { f32x2 v = {a, b}; return __builtin_bit_cast(unsigned, __builtin_convertvector(v, bf16x2)); }
DEVI float blo(unsigned u) { return __builtin_bit_cast(float, u << 16); }
DEVI float bhi(unsigned u) { return __builtin_bit_cast(float, u & 0xffff0000u); }
DEVI rsrc_t mkrsrc(const void* p, unsigned bytes) { return __builtin_amdgcn_make_buffer_rsrc((void*)p, 0, bytes, 0x00020000); }
DEVI void dma16(rsrc_t rs, unsigned voff, unsigned soff, unsigned lds) {
    unsigned keep;
    asm volatile("s_mov_b32 %0, m0\n\ts_mov_b32 m0, %1\n\ts_nop 4\n\tbuffer_load_dwordx4 %2, %3, %4 offen lds\n\ts_mov_b32 m0, %0"
                 : "=&s"(keep) : "s"(lds), "v"(voff), "s"(rs), "s"(soff) : "memory");
}
#define WAITV(n) asm volatile("s_waitcnt vmcnt(" #n ")" ::: "memory")
#define WAITL() asm volatile("s_waitcnt lgkmcnt(0)" ::: "memory")
#define BAR() asm volatile("s_barrier" ::: "memory")
DEVI u32x4 lds128(unsigned a) { return *(LDS3 u32x4*)(size_t)a; }
DEVI void sts128(unsigned a, u32x4 v) { *(LDS3 u32x4*)(size_t)a = v; }
DEVI f32x2 swp32(float x) { float y = x; asm("s_nop 1\n\tv_permlane32_swap_b32 %0, %1\n\ts_nop 1" : "+v"(x), "+v"(y)); return f32x2{x, y}; }
DEVI f32x2 swp16(float x) { float y = x; asm("s_nop 1\n\tv_permlane16_swap_b32 %0, %1\n\ts_nop 1" : "+v"(x), "+v"(y)); return f32x2{x, y}; }
DEVI float xsum32(float x) { const f32x2 t = swp32(x); return t[0] + t[1]; }
DEVI float xmax32(float x) { const f32x2 t = swp32(x); return fmaxf(t[0], t[1]); }
DEVI float xsum16(float x) { const f32x2 t = swp16(x); return t[0] + t[1]; }
DEVI float xmax16(float x) { const f32x2 t = swp16(x); return fmaxf(t[0], t[1]); }
template <int C> DEVI float dppf(float x) { return __builtin_bit_cast(float, __builtin_amdgcn_update_dpp(0, __builtin_bit_cast(int, x), C, 0xf, 0xf, true)); }
DEVI float rowsum16(float x) { x += dppf<0xB1>(x); x += dppf<0x4E>(x); x += dppf<0x141>(x); x += dppf<0x140>(x); return x; }
DEVI float rowmax16(float x) { x = fmaxf(x, dppf<0xB1>(x)); x = fmaxf(x, dppf<0x4E>(x)); x = fmaxf(x, dppf<0x141>(x)); x = fmaxf(x, dppf<0x140>(x)); return x; }
DEVI float wavesum(float x) { return xsum32(xsum16(rowsum16(x))); }
DEVI float wavemax(float x) { return xmax32(xmax16(rowmax16(x))); }
DEVI float dot2(unsigned a, unsigned b, float c) { return __builtin_amdgcn_fdot2_f32_bf16(__builtin_bit_cast(bf16x2, a), __builtin_bit_cast(bf16x2, b), c, false); }

#define LDW(voff, soff) __builtin_amdgcn_raw_buffer_load_b128(rw, voff, soff, 0)
#define LDC(voff, soff) __builtin_amdgcn_raw_buffer_load_b128(rw, voff, soff, 16)
DEVI void stc(u32x4 v, rsrc_t rs, unsigned voff, unsigned soff) { __builtin_amdgcn_raw_buffer_store_b128(v, rs, voff, soff, 16); asm volatile("s_nop 1" :: "v"(v) : "memory"); }
#define STC(v, voff, soff) stc(v, rw, voff, soff)
constexpr unsigned L1_X = 0, L1_WQ = 65536, L1_WKV = 131072, L1_PK = 147456, L1_END = 163840;
DEVI bf16x8 ldtr(unsigned a, unsigned b) {
    const s16x4 lo = __builtin_amdgcn_ds_read_tr16_b64_v4i16((LDS3 s16x4*)(size_t)a), hi = __builtin_amdgcn_ds_read_tr16_b64_v4i16((LDS3 s16x4*)(size_t)b);
    return B8(__builtin_shufflevector(lo, hi, 0, 1, 2, 3, 4, 5, 6, 7));
}
__global__ __launch_bounds__(512, 2) void k1_kernel(const unsigned short* __restrict__ x, const unsigned short* __restrict__ wq,
        const unsigned short* __restrict__ wk, const unsigned short* __restrict__ wv, const unsigned short* __restrict__ wo,
        const unsigned short* __restrict__ w1, const unsigned short* __restrict__ w2, char* __restrict__ ws) {
    __shared__ __align__(16) char smem[L1_END];
    const unsigned sb = (unsigned)(size_t)(LDS3 char*)smem;
    const int tid = threadIdx.x, lane = tid & 63, w = __builtin_amdgcn_readfirstlane(tid >> 6);
    const int b = blockIdx.x, g = (b & 7) * 4 + (b >> 6), j = (b >> 3) & 7;
    const int rl = lane >> 3, pl = lane & 7;
    const rsrc_t rx = mkrsrc(x, 2048 * 512 * 2), rq = mkrsrc(wq, 512 * 512 * 2), rkv = mkrsrc((j & 1) ? wv : wk, 512 * 16 * 2);
    STAMP1DECL STAMP1(0);
    u32x4 xr[8];
    {
        const unsigned vo = rl * 1024 + pl * 16, so = (g * 64 + w * 8) * 1024;
        asm volatile("s_nop 4\n\tbuffer_load_dwordx4 %0, %8, %9, %10 offen\n\tbuffer_load_dwordx4 %1, %8, %9, %10 offen offset:128\n\t"
                     "buffer_load_dwordx4 %2, %8, %9, %10 offen offset:256\n\tbuffer_load_dwordx4 %3, %8, %9, %10 offen offset:384\n\t"
                     "buffer_load_dwordx4 %4, %8, %9, %10 offen offset:512\n\tbuffer_load_dwordx4 %5, %8, %9, %10 offen offset:640\n\t"
                     "buffer_load_dwordx4 %6, %8, %9, %10 offen offset:768\n\tbuffer_load_dwordx4 %7, %8, %9, %10 offen offset:896"
                     : "=&v"(xr[0]), "=&v"(xr[1]), "=&v"(xr[2]), "=&v"(xr[3]), "=&v"(xr[4]), "=&v"(xr[5]), "=&v"(xr[6]), "=&v"(xr[7])
                     : "v"(vo), "s"(rx), "s"(so) : "memory");
    }
#pragma unroll
    for (int i = 0; i < 8; i++)
        dma16(rq, rl * 1024 + (((pl >> 1) ^ (((rl >> 1) & 1) | ((i & 1) << 1))) * 32) + (pl & 1) * 16, (64 * w + 8 * i) * 1024 + j * 128, sb + L1_WQ + (w * 8 + i) * 1024);
#pragma unroll
    for (int i = 0; i < 2; i++) dma16(rkv, lane * 16, (w * 2 + i) * 1024, sb + L1_WKV + (w * 2 + i) * 1024);
    asm volatile("s_waitcnt vmcnt(10)" : "+v"(xr[0]), "+v"(xr[1]), "+v"(xr[2]), "+v"(xr[3]), "+v"(xr[4]), "+v"(xr[5]), "+v"(xr[6]), "+v"(xr[7]) :: "memory");
    {
        float s0 = 0.f, s1 = 0.f, q0 = 0.f, q1 = 0.f;
#pragma unroll
        for (int i = 0; i < 8; i++)
#pragma unroll
            for (int e = 0; e < 4; e += 2) {
                s0 = dot2(xr[i][e], 0x3f803f80u, s0); q0 = dot2(xr[i][e], xr[i][e], q0);
                s1 = dot2(xr[i][e + 1], 0x3f803f80u, s1); q1 = dot2(xr[i][e + 1], xr[i][e + 1], q1);
            }
        float s = s0 + s1, q = q0 + q1;
        s += dppf<0xB1>(s); q += dppf<0xB1>(q); s += dppf<0x4E>(s); q += dppf<0x4E>(q); s += dppf<0x141>(s); q += dppf<0x141>(q);
        s *= (1.f / 512.f); q *= (1.f / 512.f);
        const float rstd = __builtin_amdgcn_rsqf(fmaxf(q - s * s, 0.f) + 1e-5f), nm = -s * rstd;
        const int row = w * 8 + rl;
#pragma unroll
        for (int i = 0; i < 8; i++) {
            u32x4 o;
#pragma unroll
            for (int e = 0; e < 4; e++) o[e] = cvtpk(fmaf(blo(xr[i][e]), rstd, nm), fmaf(bhi(xr[i][e]), rstd, nm));
            sts128(sb + L1_X + row * 1024 + (((8 * i + pl) ^ (row & 15)) * 16), o);
        }
    }
    unsigned pd0, pd1;
    {
        const int id = b * 8 + w;
        const unsigned short* src; int ld, k0, n0; unsigned bytes;
        if (id < 256)       { src = wo; ld = 512;  bytes = 512 * 512 * 2;  k0 = (id & 31) * 16; n0 = (id >> 5) * 64; pd0 = WO_OFF + ((id >> 5) * 64 + (id & 31)) * 1024; pd1 = pd0 + 32768; }
        else if (id < 1152) { const int i2 = id - 256; src = w1; ld = 1792; bytes = 512 * 1792 * 2; k0 = (i2 & 31) * 16; n0 = (i2 >> 5) * 64;
                              pd0 = W1_OFF + ((i2 >> 5) * 64 + (i2 & 31)) * 1024; pd1 = pd0 + 32768; }
        else                { const int i2 = id - 1152, jc = i2 / 112, fk = i2 - jc * 112;
                              src = w2; ld = 512; bytes = 512 * 1792 * 2; k0 = fk * 16; n0 = jc * 64; pd0 = W2_OFF + i2 * 2048; pd1 = pd0 + 1024; }
        const rsrc_t rp = mkrsrc(src, bytes);
#pragma unroll
        for (int i = 0; i < 2; i++) dma16(rp, rl * ld * 2 + pl * 16, ((k0 + 8 * i) * ld + n0) * 2, sb + L1_PK + w * 2048 + i * 1024);
    }
    WAITV(2); WAITL(); BAR();
    STAMP1(2);
    const int c = lane >> 4, li = lane & 15, hq = w & 3, tt = w >> 2;
    const rsrc_t rw = mkrsrc(ws, WS_END);
    STC((u32x4{~0u, ~0u, ~0u, ~0u}), lane * 16, O_OFF + (b * 8 + w) * 1024);
    const unsigned arow = 8 * c + (li >> 2);
    const unsigned abase = sb + L1_WQ + arow * 128 + ((hq ^ (((li >> 3) & 1) | ((c & 1) << 1))) * 32) + (li & 3) * 8;
    unsigned bb[4];
#pragma unroll
    for (int m = 0; m < 4; m++) bb[m] = sb + L1_X + li * 1024 + (((4 * m + c) ^ li) * 16);
    const int sub = j >> 1;
    {
        const unsigned wb = sb + L1_WKV + arow * 32 + (li & 3) * 8 + w * 2048;
        const unsigned xb = sb + L1_X + (sub * 16 + li) * 1024 + (w >> 1) * 256;
        f32x4 d = {0, 0, 0, 0};
#pragma unroll
        for (int u = 0; u < 2; u++) {
            const bf16x8 wf = ldtr(wb + u * 1024, wb + u * 1024 + 128);
            const bf16x8 xf = B8(lds128(xb + (((8 * (w & 1) + 4 * u + c) ^ li) * 16)));
            if (j & 1) d = MF16(xf, wf, d); else d = MF16(wf, xf, d);
        }
        *(LDS3 f32x4*)(size_t)(sb + L1_WKV + w * 2048 + lane * 16) = d;
    }
    f32x4 a0 = {0, 0, 0, 0}, a1 = {0, 0, 0, 0};
    {
        bf16x8 af[3]; u32x4 b0[3], b1[3];
#define K1LD(ks) { af[(ks) % 3] = ldtr(abase + (ks) * 4096, abase + (ks) * 4096 + 512); const unsigned ba = bb[(ks) & 3] + tt * 32768 + ((ks) >> 2) * 256; \
                   b0[(ks) % 3] = lds128(ba); b1[(ks) % 3] = lds128(ba + 16384); SB; }
        K1LD(0) K1LD(1) K1LD(2)
#pragma unroll
        for (int ks = 0; ks < 16; ks++) {
            a0 = MF16(af[ks % 3], B8(b0[ks % 3]), a0); a1 = MF16(af[ks % 3], B8(b1[ks % 3]), a1); SB;
            if (ks < 13) K1LD(ks + 3)
        }
    }
    {
        const float sc = 0.25f * LOG2E;
        const u32x2 s0 = __builtin_amdgcn_permlane32_swap(cvtpk(a0[0] * sc, a0[1] * sc), cvtpk(a1[0] * sc, a1[1] * sc), false, false);
        const u32x2 s1 = __builtin_amdgcn_permlane32_swap(cvtpk(a0[2] * sc, a0[3] * sc), cvtpk(a1[2] * sc, a1[3] * sc), false, false);
        STAMP1(1);
        STC((u32x4{s0[0], s1[0], s0[1], s1[1]}), ((c & 1) * 32 + (c >> 1) * 16 + li) * 16, Q_OFF + ((g * 2 + tt) * 32 + 4 * j + hq) * 1024);
        WAITV(2);
        const unsigned pa = sb + L1_PK + w * 2048 + (4 * (c >> 1) + (li >> 2)) * 128 + (c & 1) * 32 + (li & 3) * 8;
        STC(__builtin_bit_cast(u32x4, ldtr(pa, pa + 1024)), lane * 16, pd0);
        STC(__builtin_bit_cast(u32x4, ldtr(pa + 64, pa + 1088)), lane * 16, pd1);
    }
    WAITL(); BAR();
    if (w == 0) {
        f32x4 d = {0, 0, 0, 0};
#pragma unroll
        for (int i = 0; i < 8; i++) d += *(LDS3 f32x4*)(size_t)(sb + L1_WKV + i * 2048 + lane * 16);
        const u32x2 o = {cvtpk(d[0], d[1]), cvtpk(d[2], d[3])};
        const int s = sub & 1;
        char* kv = ws + KV_OFF + (g * 2 + (sub >> 1)) * 2048;
        if (j & 1) *(u32x2*)(kv + 1024 + s * 512 + (c & 1) * 256 + li * 16 + (c >> 1) * 8) = o;
        else {
            *(u32x2*)(kv + ((c & 1) * 32 + 16 * s + li) * 16 + (c >> 1) * 8) = o;
            const float n2 = rowmax16(xsum32(xsum16(dot2(o[0], o[0], dot2(o[1], o[1], 0.f)))));
            if (lane == 0) ((float*)(ws + KMAX_OFF))[g * 4 + sub] = n2;
        }
    }
    STAMP1(3);
}

constexpr unsigned L2_STAT = 131072, L2_ONES = 135168, L2_X = 139264, L2_W2 = 139264, L2_END = 163840;
DEVI u32x4 clean(u32x4 v) { return u32x4{min(v[0], 0xfffffffeu), min(v[1], 0xfffffffeu), min(v[2], 0xfffffffeu), min(v[3], 0xfffffffeu)}; }
template <int I> DEVI void poll_issue(u32x4& f, unsigned pend, rsrc_t rw, unsigned voff, unsigned soff) {
    asm volatile("s_bitcmp1_b32 %1, %5\n\ts_cbranch_scc0 .Lskip%=\n\tbuffer_load_dwordx4 %0, %2, %3, %4 offen sc1\n.Lskip%=:"
                 : "+v"(f) : "s"(pend), "v"(voff), "s"(rw), "s"(soff), "n"(I) : "memory", "scc");
}
DEVI void poll8(u32x4 (&f)[8], rsrc_t rw, unsigned voff, unsigned base, unsigned stride) {
    unsigned pend = 0xffu;
#pragma unroll
    for (int i = 0; i < 8; i++) f[i] = u32x4{0, 0, 0, 0};
    for (unsigned spins = 0; spins < (1u << 18); spins++) {
        asm volatile("s_nop 4" ::: "memory");
        poll_issue<0>(f[0], pend, rw, voff, base); poll_issue<1>(f[1], pend, rw, voff, base + stride);
        poll_issue<2>(f[2], pend, rw, voff, base + 2 * stride); poll_issue<3>(f[3], pend, rw, voff, base + 3 * stride);
        poll_issue<4>(f[4], pend, rw, voff, base + 4 * stride); poll_issue<5>(f[5], pend, rw, voff, base + 5 * stride);
        poll_issue<6>(f[6], pend, rw, voff, base + 6 * stride); poll_issue<7>(f[7], pend, rw, voff, base + 7 * stride);
        asm volatile("s_waitcnt vmcnt(0)" : "+v"(f[0]), "+v"(f[1]), "+v"(f[2]), "+v"(f[3]), "+v"(f[4]), "+v"(f[5]), "+v"(f[6]), "+v"(f[7]) :: "memory");
        unsigned np = 0;
#pragma unroll
        for (int i = 0; i < 8; i++) np |= __any(max(max(f[i][0], f[i][1]), max(f[i][2], f[i][3])) == ~0u) ? (1u << i) : 0u;
        pend = __builtin_amdgcn_readfirstlane(np);
        if (pend == 0) break;
    }
}

#define LD7 "s_nop 4\n\tbuffer_load_dwordx4 %0, %7, %8, %9 offen sc1\n\tbuffer_load_dwordx4 %1, %7, %8, %9 offen offset:1024 sc1\n\t"   \
            "buffer_load_dwordx4 %2, %7, %8, %9 offen offset:2048 sc1\n\tbuffer_load_dwordx4 %3, %7, %8, %9 offen offset:3072 sc1\n\t"    \
            "buffer_load_dwordx4 %4, %7, %8, %10 offen sc1\n\tbuffer_load_dwordx4 %5, %7, %8, %10 offen offset:1024 sc1\n\t"              \
            "buffer_load_dwordx4 %6, %7, %8, %10 offen offset:2048 sc1"
DEVI void issue7(u32x4 (&f)[7], rsrc_t rw, unsigned voff, unsigned soff) {
    asm volatile(LD7 : "=&v"(f[0]), "=&v"(f[1]), "=&v"(f[2]), "=&v"(f[3]), "=&v"(f[4]), "=&v"(f[5]), "=&v"(f[6])
                 : "v"(voff), "s"(rw), "s"(soff), "s"(soff + 4096) : "memory");
}
template <int N> DEVI void check7(u32x4 (&f)[7], rsrc_t rw, unsigned voff, unsigned soff) {
    asm volatile("s_waitcnt vmcnt(%7)" : "+v"(f[0]), "+v"(f[1]), "+v"(f[2]), "+v"(f[3]), "+v"(f[4]), "+v"(f[5]), "+v"(f[6]) : "n"(N) : "memory");
#pragma unroll 1
    for (unsigned spins = 0; spins < (1u << 18); spins++) {
        unsigned m = 0;
#pragma unroll
        for (int i = 0; i < 7; i++) m = max(max(m, max(f[i][0], f[i][1])), max(f[i][2], f[i][3]));
        if (!__any(m == ~0u)) break;
        asm volatile(LD7 "\n\ts_waitcnt vmcnt(0)" : "+v"(f[0]), "+v"(f[1]), "+v"(f[2]), "+v"(f[3]), "+v"(f[4]), "+v"(f[5]), "+v"(f[6])
                     : "v"(voff), "s"(rw), "s"(soff), "s"(soff + 4096) : "memory");
    }
}

__global__ __launch_bounds__(512, 2) void k2_kernel(const unsigned short* __restrict__ x, const unsigned short* __restrict__ b1,
                                                    unsigned short* __restrict__ out, char* ws) {
    __shared__ __align__(16) char smem[L2_END];
    const unsigned sb = (unsigned)(size_t)(LDS3 char*)smem;
    const int tid = threadIdx.x, lane = tid & 63, w = __builtin_amdgcn_readfirstlane(tid >> 6);
    const int b = blockIdx.x, g = (b & 7) * 4 + (b >> 6), j = (b >> 3) & 7;
    const int r = lane & 31, h = lane >> 5, kh = w >> 2;
    const unsigned l16 = lane * 16;
    const rsrc_t rw = mkrsrc(ws, WS_END);
    STAMPDECL STAMP(0);
    u32x4 q0, q1; u32x2 km;
    asm volatile("s_nop 4\n\tbuffer_load_dwordx4 %0, %3, %4, %5 offen\n\tbuffer_load_dwordx4 %1, %3, %4, %6 offen\n\tbuffer_load_dwordx2 %2, %7, %4, 0 offen"
                 : "=&v"(q0), "=&v"(q1), "=&v"(km)
                 : "v"(l16), "s"(rw), "s"(Q_OFF + ((g * 2) * 32 + 4 * j + (w & 3)) * 1024), "s"(Q_OFF + ((g * 2 + 1) * 32 + 4 * j + (w & 3)) * 1024),
                   "v"(KMAX_OFF + lane * 8) : "memory");
#define KVCHUNK(off, n) _Pragma("unroll") for (int i = 0; i < n; i++) { const unsigned o_ = kh * 65536 + (off) + ((w & 3) * n + i) * 1024; dma16(rw, l16, KV_OFF + o_, sb + o_); }
#define WOPIECE(i) dma16(rw, l16, WO_OFF + (j * 64 + w * 8 + (i)) * 1024, sb + kh * 65536 + (w & 3) * 8192 + (i) * 1024);
    KVCHUNK(0, 2)
    BAR();
    KVCHUNK(8192, 4)
    if (tid < 256) sts128(sb + L2_ONES + tid * 16, u32x4{0x3f803f80u, 0x3f803f80u, 0x3f803f80u, 0x3f803f80u});
    asm volatile("s_waitcnt vmcnt(6)" : "+v"(q0), "+v"(q1), "+v"(km) :: "memory");
    float qn0 = 0.f, qn1 = 0.f;
#pragma unroll
    for (int e = 0; e < 4; e++) { qn0 = dot2(q0[e], q0[e], qn0); qn1 = dot2(q1[e], q1[e], qn1); }
    const float kmax2 = wavemax(fmaxf(__builtin_bit_cast(float, km[0]), __builtin_bit_cast(float, km[1])));
    const float mb = __builtin_sqrtf(fmaxf(xsum32(qn0), xsum32(qn1)) * kmax2) * 1.001f;
    const bool fast = __all(mb <= 60.f);
    const unsigned aslot = A_OFF + ((g * 8 + j) * 28 + 2 * (w < 7 ? w : 6)) * 1024;
    const rsrc_t ra = mkrsrc(ws, w < 7 ? WS_END : 0);
#define STA(v, soff) stc(v, ra, l16, soff)
#define MARK(i) if ((i) < 4) STA((u32x4{~0u, ~0u, ~0u, ~0u}), aslot + (((i) >> 1) * 14 + ((i) & 1)) * 1024); else STC((u32x4{~0u, ~0u, ~0u, ~0u}), l16, hslot);
    const int tt = w & 1, ct = (w >> 1) & 1;
    const unsigned hslot = H_OFF + (g * 64 + 8 * j + 4 * tt + 2 * ct + kh) * 1024;
    const unsigned w1o = W1_OFF + ((7 * j + (w < 7 ? w : 6)) * 32) * 1024;
    u32x4 w1f[32];
    f32x16 o0, o1;
    float m0, m1;
#pragma unroll
    for (int i = 0; i < 16; i++) { o0[i] = 0.f; o1[i] = 0.f; }
    unsigned ka = sb + kh * 65536 + l16, va = r < 16 ? sb + kh * 65536 + 1024 + h * 256 + r * 16 : sb + L2_ONES + ((r >= 20 && r < 28) ? 64 : 0);
    const unsigned vinc = r < 16 ? 2048 : 0;
    if (fast) {
        f32x16 nm, sA, sB;
#pragma unroll
        for (int i = 0; i < 16; i++) nm[i] = -mb;
        m0 = mb; m1 = mb;
        u32x4 p1 = {0, 0, 0, 0}, k0, k1, va0, va1, vb0, vb1;
#define EY(S, i) e[i] = __builtin_amdgcn_exp2f(S[i]); SB;
#define CV(P, k, i) P[k] = cvtpk(e[i], e[i + 1]); SB;
#define STEP(S_, OP, VP1, SN, KN, QN, OC, VC0, LD) { float e[16]; u32x4 p0;                                                   \
        EY(S_, 0) EY(S_, 1) OP = MF32(B8(VP1), B8(p1), OP); SB; EY(S_, 2) EY(S_, 3) CV(p0, 0, 0) EY(S_, 4) EY(S_, 5) CV(p0, 1, 2) EY(S_, 6) \
        SN = MF32(B8(KN), B8(QN), nm); SB; LD EY(S_, 7) CV(p0, 2, 4) EY(S_, 8) EY(S_, 9) CV(p0, 3, 6) EY(S_, 10) EY(S_, 11)       \
        OC = MF32(B8(VC0), B8(p0), OC); SB; CV(p1, 0, 8) EY(S_, 12) EY(S_, 13) CV(p1, 1, 10) EY(S_, 14) EY(S_, 15) CV(p1, 2, 12) CV(p1, 3, 14) }
#define LDKV(K_, V0_, V1_, T) K_ = lds128(ka + (T) * 2048); V0_ = lds128(va); V1_ = lds128(va + 512); va += vinc; asm volatile("" : "+v"(va)); SB;
        WAITV(4); WAITL(); BAR();
        STAMP(11);
        LDKV(k0, va0, va1, 0)
        vb1 = va1;
        sA = MF32(B8(k0), B8(q0), nm);
#pragma unroll
        for (int it = 0; it < 16; it++) {
            if (it == 1) { WAITV(5); BAR(); }
            if (it == 5) { WAITV(14); BAR(); }
            if (it == 10) { WAITV(21); BAR(); }
            SB;
            STEP(sA, o1, vb1, sB, k0, q1, o0, va0, LDKV(k1, vb0, vb1, 2 * it + 1))
            if (it == 0) { KVCHUNK(24576, 5) } if (it == 1) { KVCHUNK(45056, 5) }
            if (it >= 2 && it < 14) { w1f[2 * it - 4] = LDW(l16, w1o + (2 * it - 4) * 1024); SB; }
            STEP(sB, o0, va1, sA, k1, q0, o1, va0, )
            if (it >= 2 && it < 7) { MARK(it - 2) SB; }
            if (it >= 10 && it < 14) { WOPIECE(2 * it - 20) WOPIECE(2 * it - 19) }
            STEP(sA, o1, va1, sB, k1, q1, o0, vb0, LDKV(k0, va0, va1, 2 * it + 2))
            if (it >= 2 && it < 14) { w1f[2 * it - 3] = LDW(l16, w1o + (2 * it - 3) * 1024); SB; }
            STEP(sB, o0, vb1, sA, k0, q0, o1, vb0, )
        }
        o1 = MF32(B8(vb1), B8(p1), o1);
    } else {
        KVCHUNK(24576, 5) KVCHUNK(45056, 5)
        WAITV(0); WAITL(); BAR();
        m0 = -3.0e38f; m1 = -3.0e38f;
        const f32x16 z = {0, 0, 0, 0, 0, 0, 0, 0, 0, 0, 0, 0, 0, 0, 0, 0};
#define SLOW(Q_, O_, M_) { f32x16 s = MF32(B8(kf), B8(Q_), z); float mx = s[0];                                               \
            _Pragma("unroll") for (int i = 1; i < 16; i++) mx = fmaxf(mx, s[i]);                                              \
            mx = fmaxf(xmax32(mx), M_); const float al = __builtin_amdgcn_exp2f(M_ - mx); M_ = mx; u32x4 p0, p1;              \
            _Pragma("unroll") for (int i = 0; i < 16; i++) O_[i] *= al;                                                       \
            _Pragma("unroll") for (int i = 0; i < 4; i++) {                                                                   \
                p0[i] = cvtpk(__builtin_amdgcn_exp2f(s[2 * i] - mx), __builtin_amdgcn_exp2f(s[2 * i + 1] - mx));              \
                p1[i] = cvtpk(__builtin_amdgcn_exp2f(s[2 * i + 8] - mx), __builtin_amdgcn_exp2f(s[2 * i + 9] - mx)); }        \
            O_ = MF32(B8(v0), B8(p0), O_); O_ = MF32(B8(v1), B8(p1), O_); }
#pragma unroll 1
        for (int t = 0; t < 32; t++) {
            if (t == 2 || t == 10 || t == 20) { WAITL(); BAR(); }
            if (t == 20) { _Pragma("unroll") for (int i = 0; i < 8; i++) WOPIECE(i) }
            const u32x4 kf = lds128(ka), v0 = lds128(va), v1 = lds128(va + 512);
            ka += 2048; va += vinc;
            SLOW(q0, o0, m0) SLOW(q1, o1, m1)
        }
#pragma unroll
        for (int i = 0; i < 24; i++) w1f[i] = LDW(l16, w1o + i * 1024);
#pragma unroll
        for (int i = 0; i < 5; i++) { MARK(i) }
    }
    STAMP(1);
    {
        const f32x16 os = kh ? o0 : o1, ok = kh ? o1 : o0;
        const float ms = kh ? m0 : m1, mk = kh ? m1 : m0;
        const unsigned xa = sb + L2_X + w * 2560, xb = sb + L2_X + (w ^ 4) * 2560;
        *(LDS3 f32x4*)(size_t)(xa + l16) = f32x4{os[0], os[1], os[2], os[3]};
        *(LDS3 f32x4*)(size_t)(xa + 1024 + l16) = f32x4{os[4], os[5], os[6], os[7]};
        *(LDS3 f32x2*)(size_t)(xa + 2048 + lane * 8) = f32x2{os[8], ms};
        WAITV(0); WAITL(); BAR();
        const f32x4 a = *(LDS3 f32x4*)(size_t)(xb + l16), c = *(LDS3 f32x4*)(size_t)(xb + 1024 + l16);
        const f32x2 lm = *(LDS3 f32x2*)(size_t)(xb + 2048 + lane * 8);
        const float mm = fmaxf(mk, lm[1]), ea = __builtin_amdgcn_exp2f(mk - mm), eb = __builtin_amdgcn_exp2f(lm[1] - mm);
        const float inv = 1.f / (ok[8] * ea + lm[0] * eb), fa = ea * inv, fb = eb * inv;
        u32x4 of;
        of[0] = cvtpk(ok[0] * fa + a[0] * fb, ok[1] * fa + a[1] * fb); of[1] = cvtpk(ok[2] * fa + a[2] * fb, ok[3] * fa + a[3] * fb);
        of[2] = cvtpk(ok[4] * fa + c[0] * fb, ok[5] * fa + c[1] * fb); of[3] = cvtpk(ok[6] * fa + c[2] * fb, ok[7] * fa + c[3] * fb);
        STC(clean(of), l16, O_OFF + ((g * 2 + kh) * 32 + 4 * j + (w & 3)) * 1024);
    }
    STAMP(2);
    const unsigned xo = (64 * g + 32 * tt + r) * 512 + 64 * j + 32 * ct + 16 * kh;
    const u32x2 xa0 = *(const u32x2*)(x + xo + 4 * h), xa1 = *(const u32x2*)(x + xo + 8 + 4 * h);
    u32x2 bb[4];
#pragma unroll
    for (int i = 0; i < 4; i++) bb[i] = *(const u32x2*)(b1 + 224 * j + 32 * (w < 7 ? w : 6) + 8 * i + 4 * h);
#pragma unroll
    for (int i = 24; i < 32; i++) w1f[i] = LDW(l16, w1o + i * 1024);
    const f32x16 z16 = {0, 0, 0, 0, 0, 0, 0, 0, 0, 0, 0, 0, 0, 0, 0, 0};
    const unsigned myr = sb + kh * 65536 + 32768 + (w & 3) * 8192;
    const unsigned w2o = W2_OFF + (j * 224 + w * 28) * 1024;
    float hv[8];
    {
        const int kq = w >> 1;
        u32x4 of[8], af[4];
        const unsigned wa = sb + kq * 8192 + l16;
        af[0] = lds128(wa); af[1] = lds128(wa + 65536); af[2] = lds128(wa + 1024); af[3] = lds128(wa + 65536 + 1024);
        poll8(of, rw, l16, O_OFF + ((g * 2 + tt) * 32 + 8 * kq) * 1024, 1024);
        asm volatile("" : "+v"(w1f[24]), "+v"(w1f[25]), "+v"(w1f[26]), "+v"(w1f[27]), "+v"(w1f[28]), "+v"(w1f[29]), "+v"(w1f[30]), "+v"(w1f[31]));
        STAMP(3);
        f32x16 acc[2] = {z16, z16};
#pragma unroll
        for (int i = 0; i < 16; i++) {
            acc[i & 1] = MF32(B8(af[i & 3]), B8(of[i >> 1]), acc[i & 1]); SB;
            if (i < 12) { af[i & 3] = lds128(wa + (i & 1) * 65536 + ((i >> 1) + 2) * 1024); SB; }
        }
#pragma unroll
        for (int p = 0; p < 4; p++) {
            const unsigned xa = sb + (p >> 1) * 65536 + 32768 + (tt + 2 * (p & 1)) * 8192 + kq * 2048 + l16, o = (p >> 1) * 8;
            const f32x16 v = acc[p & 1];
            *(LDS3 f32x4*)(size_t)xa = f32x4{v[o], v[o + 1], v[o + 2], v[o + 3]};
            *(LDS3 f32x4*)(size_t)(xa + 1024) = f32x4{v[o + 4], v[o + 5], v[o + 6], v[o + 7]};
        }
        WAITL(); BAR();
        f32x4 a = {blo(xa0[0]), bhi(xa0[0]), blo(xa0[1]), bhi(xa0[1])}, c = {blo(xa1[0]), bhi(xa1[0]), blo(xa1[1]), bhi(xa1[1])};
#pragma unroll
        for (int p = 0; p < 4; p++) { a += *(LDS3 f32x4*)(size_t)(myr + p * 2048 + l16); c += *(LDS3 f32x4*)(size_t)(myr + p * 2048 + 1024 + l16); }
#pragma unroll
        for (int i = 0; i < 4; i++) { hv[i] = a[i]; hv[4 + i] = c[i]; }
        STC(clean(u32x4{cvtpk(hv[0], hv[1]), cvtpk(hv[2], hv[3]), cvtpk(hv[4], hv[5]), cvtpk(hv[6], hv[7])}), l16, hslot);
    }
    STAMP(4);
#pragma unroll
    for (int i = 0; i < 11; i++) dma16(rw, l16, w2o + i * 1024, i < 8 ? myr + i * 1024 : sb + L2_W2 + (w * 3 + i - 8) * 1024);
    f32x16 cs = z16;
#pragma unroll
    for (int ks = 0; ks < 32; ks++) cs = MF32(B8(w1f[ks]), B8((u32x4{0x3f803f80u, 0x3f803f80u, 0x3f803f80u, 0x3f803f80u})), cs);
    asm volatile("" : "+v"(cs));
    {
        u32x4 hf[8];
        poll8(hf, rw, l16, H_OFF + (g * 64 + 8 * w) * 1024, 1024);
        STAMP(5);
#pragma unroll
        for (int i = 0; i < 8; i++) sts128(sb + (i >> 2) * 65536 + (4 * w + (i & 3)) * 1024 + l16, hf[i]);
        f32x4 st;
#pragma unroll
        for (int t = 0; t < 2; t++) {
            float s0 = 0.f, q0_ = 0.f, s1 = 0.f, q1_ = 0.f;
#pragma unroll
            for (int i = 0; i < 4; i++)
#pragma unroll
                for (int e = 0; e < 4; e += 2) {
                    s0 = dot2(hf[4 * t + i][e], 0x3f803f80u, s0); q0_ = dot2(hf[4 * t + i][e], hf[4 * t + i][e], q0_);
                    s1 = dot2(hf[4 * t + i][e + 1], 0x3f803f80u, s1); q1_ = dot2(hf[4 * t + i][e + 1], hf[4 * t + i][e + 1], q1_);
                }
            st[2 * t] = xsum32(s0 + s1); st[2 * t + 1] = xsum32(q0_ + q1_);
        }
        if (h == 0) *(LDS3 f32x4*)(size_t)(sb + L2_STAT + w * 512 + r * 16) = st;
        WAITL(); BAR();
    }
    STAMP(6);
    u32x4 w2f[17];
    {
        f32x16 a0 = z16, a1 = z16;
        u32x4 bf[4];
        unsigned ha0 = sb + l16, ha1 = sb + 65536 + l16;
        asm volatile("" : "+v"(ha1));
#pragma unroll
        for (int i = 0; i < 4; i++) bf[i] = lds128(ha0 + i * 1024);
        f32x4 st = {0, 0, 0, 0};
        float y[2], mean0, rstd0, mean1, rstd1;
        u32x4 yp;
#define MISH(A, i, mean, rstd) { const unsigned bw = bb[(i) >> 2][((i) >> 1) & 1];                                    \
            const float z = fmaf(fmaf(-mean, cs[i], A[i]), rstd, ((i) & 1) ? bhi(bw) : blo(bw));                         \
            const float e = __builtin_amdgcn_exp2f(fminf(z * LOG2E, 60.f)), n = e * (e + 2.f);     \
            y[(i) & 1] = z * (n * __builtin_amdgcn_rcpf(n + 2.f));                                                       \
            if ((i) & 1) yp[((i) >> 1) & 3] = min(cvtpk(y[0], y[1]), 0xfffffffeu); }
#define ACTST(t, s) STA(yp, aslot + ((t) * 14 + (s)) * 1024);
        SB;
#pragma unroll
        for (int ks = 0; ks < 32; ks++) {
            a0 = MF32(B8(w1f[ks]), B8(bf[ks & 3]), a0); SB;
            bf[ks & 3] = lds128((ks < 28 ? ha0 : ha1) + ((ks + 4) & 31) * 1024); SB;
            if (ks >= 4 && ks < 12) { st += *(LDS3 f32x4*)(size_t)(sb + L2_STAT + (ks - 4) * 512 + r * 16); SB; }
            if (ks == 16) {
                mean0 = st[0] * (1.f / 512.f); rstd0 = __builtin_amdgcn_rsqf(fmaxf(st[1] * (1.f / 512.f) - mean0 * mean0, 0.f) + 1e-5f);
                mean1 = st[2] * (1.f / 512.f); rstd1 = __builtin_amdgcn_rsqf(fmaxf(st[3] * (1.f / 512.f) - mean1 * mean1, 0.f) + 1e-5f); SB;
            }
        }
        STAMP(12);
#pragma unroll
        for (int ks = 0; ks < 32; ks++) {
            a1 = MF32(B8(w1f[ks]), B8(bf[ks & 3]), a1); SB;
            if (ks < 28) { bf[ks & 3] = lds128(ha1 + (ks + 4) * 1024); SB; }
            if (ks & 1) { MISH(a0, ks >> 1, mean0, rstd0) SB; }
            if (ks == 15) { ACTST(0, 0) SB; }
            if (ks == 31) { ACTST(0, 1) SB; }
            if (ks >= 2 && ks < 19) { w2f[ks - 2] = LDW(l16, w2o + (ks + 9) * 1024); SB; }
        }
        STAMP(13);
#pragma unroll
        for (int i = 0; i < 16; i++) { MISH(a1, i, mean1, rstd1) if (i == 7) { ACTST(1, 0) } if (i == 15) { ACTST(1, 1) } }
    }
    STAMP(7);
    {
        const unsigned ab = A_OFF + (g * 8 + w) * 28 * 1024;
        u32x4 fa[7], fb[7], wl[2];
        asm volatile("" : "+v"(w2f[0]), "+v"(w2f[1]), "+v"(w2f[2]), "+v"(w2f[3]), "+v"(w2f[4]), "+v"(w2f[5]), "+v"(w2f[6]), "+v"(w2f[7]), "+v"(w2f[8]),
                          "+v"(w2f[9]), "+v"(w2f[10]), "+v"(w2f[11]), "+v"(w2f[12]), "+v"(w2f[13]), "+v"(w2f[14]), "+v"(w2f[15]), "+v"(w2f[16]));
        issue7(fa, rw, l16, ab); issue7(fb, rw, l16, ab + 7168);
        unsigned wla = myr + l16, wlb = sb + L2_W2 + w * 3072 + l16;
        asm volatile("" : "+v"(wla), "+v"(wlb));
#define WLD(n) lds128((n) < 8 ? wla + (n) * 1024 : wlb + ((n) - 8) * 1024)
#define F2(F, ks0, C0, C1) _Pragma("unroll") for (int k = 0; k < 7; k++) { const int n0 = 2 * ((ks0) + k), n1 = n0 + 1;               \
            C0 = MF32(B8(n0 < 11 ? wl[0] : w2f[n0 < 11 ? 0 : n0 - 11]), B8(F[k]), C0); SB; if (n0 + 2 < 11) { wl[0] = WLD(n0 + 2); SB; } \
            C1 = MF32(B8(n1 < 11 ? wl[1] : w2f[n1 < 11 ? 0 : n1 - 11]), B8(F[k]), C1); SB; if (n1 + 2 < 11) { wl[1] = WLD(n1 + 2); SB; } }
#define PST(v, t, cc, s) sts128(sb + (t) * 65536 + ((cc) + 2 * (s)) * 8192 + w * 1024 + l16,                                          \
            u32x4{cvtpk(v[8 * (s)], v[8 * (s) + 1]), cvtpk(v[8 * (s) + 2], v[8 * (s) + 3]), cvtpk(v[8 * (s) + 4], v[8 * (s) + 5]), cvtpk(v[8 * (s) + 6], v[8 * (s) + 7])});
        f32x16 c0 = z16, c1 = z16;
        wl[0] = WLD(0); wl[1] = WLD(1);
        check7<7>(fa, rw, l16, ab);
        STAMP(14);
        F2(fa, 0, c0, c1)
        issue7(fa, rw, l16, ab + 14336);
        check7<7>(fb, rw, l16, ab + 7168);
        F2(fb, 7, c0, c1)
        issue7(fb, rw, l16, ab + 21504);
        wl[0] = WLD(0); wl[1] = WLD(1);
        BAR();
        PST(c0, 0, 0, 0) PST(c0, 0, 0, 1) PST(c1, 0, 1, 0) PST(c1, 0, 1, 1)
        c0 = z16; c1 = z16;
        check7<7>(fa, rw, l16, ab + 14336);
        STAMP(15);
        F2(fa, 0, c0, c1)
        check7<0>(fb, rw, l16, ab + 21504);
        F2(fb, 7, c0, c1)
        STAMP(8);
        PST(c0, 1, 0, 0) PST(c0, 1, 0, 1) PST(c1, 1, 1, 0) PST(c1, 1, 1, 1)
        WAITL(); BAR();
        STAMP(9);
#pragma unroll
        for (int i = 0; i < 8; i++) {
            const u32x4 pf = lds128(sb + tt * 65536 + (w >> 1) * 8192 + i * 1024 + l16);
#pragma unroll
            for (int e = 0; e < 4; e++) { hv[2 * e] += blo(pf[e]); hv[2 * e + 1] += bhi(pf[e]); }
        }
        const u32x2 s0 = __builtin_amdgcn_permlane32_swap(cvtpk(hv[0], hv[1]), cvtpk(hv[4], hv[5]), false, false);
        const u32x2 s1 = __builtin_amdgcn_permlane32_swap(cvtpk(hv[2], hv[3]), cvtpk(hv[6], hv[7]), false, false);
        *(u32x4*)(out + xo + 8 * h) = u32x4{s0[0], s1[0], s0[1], s1[1]};
    }
    STAMP(10);
}

extern "C" void kernel_launch(void* const* d_in, const int* in_sizes, int n_in, void* d_out, int out_size, void* d_ws, size_t ws_size,
                              hipStream_t stream) {
    typedef const unsigned short* P;
    k1_kernel<<<256, 512, 0, stream>>>((P)d_in[0], (P)d_in[1], (P)d_in[2], (P)d_in[3], (P)d_in[4], (P)d_in[6], (P)d_in[8], (char*)d_ws);
    k2_kernel<<<256, 512, 0, stream>>>((P)d_in[0], (P)d_in[7], (unsigned short*)d_out, (char*)d_ws);
}
```

```cpp
#include <hip/hip_runtime.h>
#include <hip/hip_bf16.h>

typedef __attribute__((ext_vector_type(8))) __bf16 bf16x8;
typedef __attribute__((ext_vector_type(4))) __bf16 bf16x4;
typedef __attribute__((ext_vector_type(2))) __bf16 bf16x2;
typedef __attribute__((ext_vector_type(16))) float f32x16;
typedef __attribute__((ext_vector_type(4))) float f32x4;
typedef __attribute__((ext_vector_type(2))) float f32x2;
typedef __attribute__((ext_vector_type(4))) unsigned u32x4;
typedef __attribute__((ext_vector_type(2))) unsigned u32x2;
typedef __attribute__((ext_vector_type(4))) short s16x4;
typedef __amdgpu_buffer_rsrc_t rsrc_t;
#define LDS3 __attribute__((address_space(3)))
#define GLB1 __attribute__((address_space(1)))
#define DEVI __device__ __forceinline__
#define SB __builtin_amdgcn_sched_barrier(0)
#define B8(x) __builtin_bit_cast(bf16x8, x)
#define MF32(a, b, c) __builtin_amdgcn_mfma_f32_32x32x16_bf16(a, b, c, 0, 0, 0)
#define MF16(a, b, c) __builtin_amdgcn_mfma_f32_16x16x32_bf16(a, b, c, 0, 0, 0)
#define STAMP(i)
#define STAMP1(i)
#define STAMP1DECL
#define STAMPDECL

constexpr unsigned FLAG_OFF = 0, KMAX_OFF = 16384, KV_OFF = 32768, Q_OFF = 0x40000, WO_OFF = Q_OFF + (2u << 20),
                   W1_OFF = WO_OFF + (512u << 10), W2_OFF = W1_OFF + (1792u << 10), O_OFF = W2_OFF + (1792u << 10),
                   H_OFF = O_OFF + (2u << 20), A_OFF = H_OFF + (2u << 20), WS_END = A_OFF + (7u << 20);
constexpr float LOG2E = 1.4426950408889634f;

template <int V> struct IC { static constexpr int v = V; };
template <int N, class F> DEVI void sfor(F&& f) { if constexpr (N > 0) { sfor<N - 1>(f); f(IC<N - 1>{}); } }
DEVI unsigned cvtpk(float a, float b) { f32x2 v = {a, b}; return __builtin_bit_cast(unsigned, __builtin_convertvector(v, bf16x2)); }
DEVI float blo(unsigned u) { return __builtin_bit_cast(float, u << 16); }
DEVI float bhi(unsigned u) { return __builtin_bit_cast(float, u & 0xffff0000u); }
DEVI rsrc_t mkrsrc(const void* p, unsigned bytes) { return __builtin_amdgcn_make_buffer_rsrc((void*)p, 0, bytes, 0x00020000); }
DEVI void dma16(rsrc_t rs, unsigned voff, unsigned soff, unsigned lds) {
    unsigned keep;
    asm volatile("s_mov_b32 %0, m0\n\ts_mov_b32 m0, %1\n\ts_nop 4\n\tbuffer_load_dwordx4 %2, %3, %4 offen lds\n\ts_mov_b32 m0, %0"
                 : "=&s"(keep) : "s"(lds), "v"(voff), "s"(rs), "s"(soff) : "memory");
}
#define WAITV(n) asm volatile("s_waitcnt vmcnt(" #n ")" ::: "memory")
#define WAITL() asm volatile("s_waitcnt lgkmcnt(0)" ::: "memory")
#define BAR() asm volatile("s_barrier" ::: "memory")
DEVI u32x4 lds128(unsigned a) { return *(LDS3 u32x4*)(size_t)a; }
DEVI void sts128(unsigned a, u32x4 v) { *(LDS3 u32x4*)(size_t)a = v; }
DEVI f32x2 swp32(float x) { float y = x; asm("s_nop 1\n\tv_permlane32_swap_b32 %0, %1\n\ts_nop 1" : "+v"(x), "+v"(y)); return f32x2{x, y}; }
DEVI f32x2 swp16(float x) { float y = x; asm("s_nop 1\n\tv_permlane16_swap_b32 %0, %1\n\ts_nop 1" : "+v"(x), "+v"(y)); return f32x2{x, y}; }
DEVI float xsum32(float x) { const f32x2 t = swp32(x); return t[0] + t[1]; }
DEVI float xmax32(float x) { const f32x2 t = swp32(x); return fmaxf(t[0], t[1]); }
DEVI float xsum16(float x) { const f32x2 t = swp16(x); return t[0] + t[1]; }
DEVI float xmax16(float x) { const f32x2 t = swp16(x); return fmaxf(t[0], t[1]); }
template <int C> DEVI float dppf(float x) { return __builtin_bit_cast(float, __builtin_amdgcn_update_dpp(0, __builtin_bit_cast(int, x), C, 0xf, 0xf, true)); }
DEVI float rowsum16(float x) { x += dppf<0xB1>(x); x += dppf<0x4E>(x); x += dppf<0x141>(x); x += dppf<0x140>(x); return x; }
DEVI float rowmax16(float x) { x = fmaxf(x, dppf<0xB1>(x)); x = fmaxf(x, dppf<0x4E>(x)); x = fmaxf(x, dppf<0x141>(x)); x = fmaxf(x, dppf<0x140>(x)); return x; }
DEVI float wavesum(float x) { return xsum32(xsum16(rowsum16(x))); }
DEVI float wavemax(float x) { return xmax32(xmax16(rowmax16(x))); }
DEVI float dot2(unsigned a, unsigned b, float c) { return __builtin_amdgcn_fdot2_f32_bf16(__builtin_bit_cast(bf16x2, a), __builtin_bit_cast(bf16x2, b), c, false); }

#define LDW(voff, soff) __builtin_amdgcn_raw_buffer_load_b128(rw, voff, soff, 0)
#define LDC(voff, soff) __builtin_amdgcn_raw_buffer_load_b128(rw, voff, soff, 16)
DEVI void stc(u32x4 v, rsrc_t rs, unsigned voff, unsigned soff) { __builtin_amdgcn_raw_buffer_store_b128(v, rs, voff, soff, 16); asm volatile("s_nop 1" :: "v"(v) : "memory"); }
#define STC(v, voff, soff) stc(v, rw, voff, soff)
constexpr unsigned L1_X = 0, L1_WQ = 65536, L1_WKV = 131072, L1_PK = 147456, L1_END = 163840;
DEVI bf16x8 ldtr(unsigned a, unsigned b) {
    const s16x4 lo = __builtin_amdgcn_ds_read_tr16_b64_v4i16((LDS3 s16x4*)(size_t)a), hi = __builtin_amdgcn_ds_read_tr16_b64_v4i16((LDS3 s16x4*)(size_t)b);
    return B8(__builtin_shufflevector(lo, hi, 0, 1, 2, 3, 4, 5, 6, 7));
}
__global__ __launch_bounds__(512, 2) void k1_kernel(const unsigned short* __restrict__ x, const unsigned short* __restrict__ wq,
        const unsigned short* __restrict__ wk, const unsigned short* __restrict__ wv, const unsigned short* __restrict__ wo,
        const unsigned short* __restrict__ w1, const unsigned short* __restrict__ w2, char* __restrict__ ws) {
    __shared__ __align__(16) char smem[L1_END];
    const unsigned sb = (unsigned)(size_t)(LDS3 char*)smem;
    const int tid = threadIdx.x, lane = tid & 63, w = __builtin_amdgcn_readfirstlane(tid >> 6);
    const int b = blockIdx.x, g = (b & 7) * 4 + (b >> 6), j = (b >> 3) & 7;
    const int rl = lane >> 3, pl = lane & 7;
    const rsrc_t rx = mkrsrc(x, 2048 * 512 * 2), rq = mkrsrc(wq, 512 * 512 * 2), rkv = mkrsrc((j & 1) ? wv : wk, 512 * 16 * 2);
    STAMP1DECL STAMP1(0);
    u32x4 xr[8];
    {
        const unsigned vo = rl * 1024 + pl * 16, so = (g * 64 + w * 8) * 1024;
        asm volatile("s_nop 4\n\tbuffer_load_dwordx4 %0, %8, %9, %10 offen\n\tbuffer_load_dwordx4 %1, %8, %9, %10 offen offset:128\n\t"
                     "buffer_load_dwordx4 %2, %8, %9, %10 offen offset:256\n\tbuffer_load_dwordx4 %3, %8, %9, %10 offen offset:384\n\t"
                     "buffer_load_dwordx4 %4, %8, %9, %10 offen offset:512\n\tbuffer_load_dwordx4 %5, %8, %9, %10 offen offset:640\n\t"
                     "buffer_load_dwordx4 %6, %8, %9, %10 offen offset:768\n\tbuffer_load_dwordx4 %7, %8, %9, %10 offen offset:896"
                     : "=&v"(xr[0]), "=&v"(xr[1]), "=&v"(xr[2]), "=&v"(xr[3]), "=&v"(xr[4]), "=&v"(xr[5]), "=&v"(xr[6]), "=&v"(xr[7])
                     : "v"(vo), "s"(rx), "s"(so) : "memory");
    }
#pragma unroll
    for (int i = 0; i < 8; i++)
        dma16(rq, rl * 1024 + (((pl >> 1) ^ (((rl >> 1) & 1) | ((i & 1) << 1))) * 32) + (pl & 1) * 16, (64 * w + 8 * i) * 1024 + j * 128, sb + L1_WQ + (w * 8 + i) * 1024);
#pragma unroll
    for (int i = 0; i < 2; i++) dma16(rkv, lane * 16, (w * 2 + i) * 1024, sb + L1_WKV + (w * 2 + i) * 1024);
    asm volatile("s_waitcnt vmcnt(10)" : "+v"(xr[0]), "+v"(xr[1]), "+v"(xr[2]), "+v"(xr[3]), "+v"(xr[4]), "+v"(xr[5]), "+v"(xr[6]), "+v"(xr[7]) :: "memory");
    {
        float s0 = 0.f, s1 = 0.f, q0 = 0.f, q1 = 0.f;
#pragma unroll
        for (int i = 0; i < 8; i++)
#pragma unroll
            for (int e = 0; e < 4; e += 2) {
                s0 = dot2(xr[i][e], 0x3f803f80u, s0); q0 = dot2(xr[i][e], xr[i][e], q0);
                s1 = dot2(xr[i][e + 1], 0x3f803f80u, s1); q1 = dot2(xr[i][e + 1], xr[i][e + 1], q1);
            }
        float s = s0 + s1, q = q0 + q1;
        s += dppf<0xB1>(s); q += dppf<0xB1>(q); s += dppf<0x4E>(s); q += dppf<0x4E>(q); s += dppf<0x141>(s); q += dppf<0x141>(q);
        s *= (1.f / 512.f); q *= (1.f / 512.f);
        const float rstd = __builtin_amdgcn_rsqf(fmaxf(q - s * s, 0.f) + 1e-5f), nm = -s * rstd;
        const int row = w * 8 + rl;
#pragma unroll
        for (int i = 0; i < 8; i++) {
            u32x4 o;
#pragma unroll
            for (int e = 0; e < 4; e++) o[e] = cvtpk(fmaf(blo(xr[i][e]), rstd, nm), fmaf(bhi(xr[i][e]), rstd, nm));
            sts128(sb + L1_X + row * 1024 + (((8 * i + pl) ^ (row & 15)) * 16), o);
        }
    }
    unsigned pd0, pd1;
    {
        const int id = b * 8 + w;
        const unsigned short* src; int ld, k0, n0; unsigned bytes;
        if (id < 256)       { src = wo; ld = 512;  bytes = 512 * 512 * 2;  k0 = (id & 31) * 16; n0 = (id >> 5) * 64; pd0 = WO_OFF + ((id >> 5) * 64 + (id & 31)) * 1024; pd1 = pd0 + 32768; }
        else if (id < 1152) { const int i2 = id - 256; src = w1; ld = 1792; bytes = 512 * 1792 * 2; k0 = (i2 & 31) * 16; n0 = (i2 >> 5) * 64;
                              pd0 = W1_OFF + ((i2 >> 5) * 64 + (i2 & 31)) * 1024; pd1 = pd0 + 32768; }
        else                { const int i2 = id - 1152, jc = i2 / 112, fk = i2 - jc * 112;
                              src = w2; ld = 512; bytes = 512 * 1792 * 2; k0 = fk * 16; n0 = jc * 64; pd0 = W2_OFF + i2 * 2048; pd1 = pd0 + 1024; }
        const rsrc_t rp = mkrsrc(src, bytes);
#pragma unroll
        for (int i = 0; i < 2; i++) dma16(rp, rl * ld * 2 + pl * 16, ((k0 + 8 * i) * ld + n0) * 2, sb + L1_PK + w * 2048 + i * 1024);
    }
    WAITV(2); WAITL(); BAR();
    STAMP1(2);
    const int c = lane >> 4, li = lane & 15, hq = w & 3, tt = w >> 2;
    const rsrc_t rw = mkrsrc(ws, WS_END);
    STC((u32x4{~0u, ~0u, ~0u, ~0u}), lane * 16, O_OFF + (b * 8 + w) * 1024);
    const unsigned arow = 8 * c + (li >> 2);
    const unsigned abase = sb + L1_WQ + arow * 128 + ((hq ^ (((li >> 3) & 1) | ((c & 1) << 1))) * 32) + (li & 3) * 8;
    unsigned bb[4];
#pragma unroll
    for (int m = 0; m < 4; m++) bb[m] = sb + L1_X + li * 1024 + (((4 * m + c) ^ li) * 16);
    const int sub = j >> 1;
    {
        const unsigned wb = sb + L1_WKV + arow * 32 + (li & 3) * 8 + w * 2048;
        const unsigned xb = sb + L1_X + (sub * 16 + li) * 1024 + (w >> 1) * 256;
        f32x4 d = {0, 0, 0, 0};
#pragma unroll
        for (int u = 0; u < 2; u++) {
            const bf16x8 wf = ldtr(wb + u * 1024, wb + u * 1024 + 128);
            const bf16x8 xf = B8(lds128(xb + (((8 * (w & 1) + 4 * u + c) ^ li) * 16)));
            if (j & 1) d = MF16(xf, wf, d); else d = MF16(wf, xf, d);
        }
        *(LDS3 f32x4*)(size_t)(sb + L1_WKV + w * 2048 + lane * 16) = d;
    }
    f32x4 a0 = {0, 0, 0, 0}, a1 = {0, 0, 0, 0};
    {
        bf16x8 af[3]; u32x4 b0[3], b1[3];
#define K1LD(ks) { af[(ks) % 3] = ldtr(abase + (ks) * 4096, abase + (ks) * 4096 + 512); const unsigned ba = bb[(ks) & 3] + tt * 32768 + ((ks) >> 2) * 256; \
                   b0[(ks) % 3] = lds128(ba); b1[(ks) % 3] = lds128(ba + 16384); SB; }
        K1LD(0) K1LD(1) K1LD(2)
#pragma unroll
        for (int ks = 0; ks < 16; ks++) {
            a0 = MF16(af[ks % 3], B8(b0[ks % 3]), a0); a1 = MF16(af[ks % 3], B8(b1[ks % 3]), a1); SB;
            if (ks < 13) K1LD(ks + 3)
        }
    }
    {
        const float sc = 0.25f * LOG2E;
        const u32x2 s0 = __builtin_amdgcn_permlane32_swap(cvtpk(a0[0] * sc, a0[1] * sc), cvtpk(a1[0] * sc, a1[1] * sc), false, false);
        const u32x2 s1 = __builtin_amdgcn_permlane32_swap(cvtpk(a0[2] * sc, a0[3] * sc), cvtpk(a1[2] * sc, a1[3] * sc), false, false);
        STAMP1(1);
        STC((u32x4{s0[0], s1[0], s0[1], s1[1]}), ((c & 1) * 32 + (c >> 1) * 16 + li) * 16, Q_OFF + ((g * 2 + tt) * 32 + 4 * j + hq) * 1024);
        WAITV(2);
        const unsigned pa = sb + L1_PK + w * 2048 + (4 * (c >> 1) + (li >> 2)) * 128 + (c & 1) * 32 + (li & 3) * 8;
        STC(__builtin_bit_cast(u32x4, ldtr(pa, pa + 1024)), lane * 16, pd0);
        STC(__builtin_bit_cast(u32x4, ldtr(pa + 64, pa + 1088)), lane * 16, pd1);
    }
    WAITL(); BAR();
    if (w == 0) {
        f32x4 d = {0, 0, 0, 0};
#pragma unroll
        for (int i = 0; i < 8; i++) d += *(LDS3 f32x4*)(size_t)(sb + L1_WKV + i * 2048 + lane * 16);
        const u32x2 o = {cvtpk(d[0], d[1]), cvtpk(d[2], d[3])};
        const int s = sub & 1;
        char* kv = ws + KV_OFF + (g * 2 + (sub >> 1)) * 2048;
        if (j & 1) *(u32x2*)(kv + 1024 + s * 512 + (c & 1) * 256 + li * 16 + (c >> 1) * 8) = o;
        else {
            *(u32x2*)(kv + ((c & 1) * 32 + 16 * s + li) * 16 + (c >> 1) * 8) = o;
            const float n2 = rowmax16(xsum32(xsum16(dot2(o[0], o[0], dot2(o[1], o[1], 0.f)))));
            if (lane == 0) ((float*)(ws + KMAX_OFF))[g * 4 + sub] = n2;
        }
    }
    STAMP1(3);
}

constexpr unsigned L2_STAT = 131072, L2_ONES = 135168, L2_X = 139264, L2_W2 = 139264, L2_END = 163840;
DEVI u32x4 clean(u32x4 v) { return u32x4{min(v[0], 0xfffffffeu), min(v[1], 0xfffffffeu), min(v[2], 0xfffffffeu), min(v[3], 0xfffffffeu)}; }
template <int I> DEVI void poll_issue(u32x4& f, unsigned pend, rsrc_t rw, unsigned voff, unsigned soff) {
    asm volatile("s_bitcmp1_b32 %1, %5\n\ts_cbranch_scc0 .Lskip%=\n\tbuffer_load_dwordx4 %0, %2, %3, %4 offen sc1\n.Lskip%=:"
                 : "+v"(f) : "s"(pend), "v"(voff), "s"(rw), "s"(soff), "n"(I) : "memory", "scc");
}
DEVI void poll8(u32x4 (&f)[8], rsrc_t rw, unsigned voff, unsigned base, unsigned stride) {
    unsigned pend = 0xffu;
#pragma unroll
    for (int i = 0; i < 8; i++) f[i] = u32x4{0, 0, 0, 0};
    for (unsigned spins = 0; spins < (1u << 18); spins++) {
        asm volatile("s_nop 4" ::: "memory");
        poll_issue<0>(f[0], pend, rw, voff, base); poll_issue<1>(f[1], pend, rw, voff, base + stride);
        poll_issue<2>(f[2], pend, rw, voff, base + 2 * stride); poll_issue<3>(f[3], pend, rw, voff, base + 3 * stride);
        poll_issue<4>(f[4], pend, rw, voff, base + 4 * stride); poll_issue<5>(f[5], pend, rw, voff, base + 5 * stride);
        poll_issue<6>(f[6], pend, rw, voff, base + 6 * stride); poll_issue<7>(f[7], pend, rw, voff, base + 7 * stride);
        asm volatile("s_waitcnt vmcnt(0)" : "+v"(f[0]), "+v"(f[1]), "+v"(f[2]), "+v"(f[3]), "+v"(f[4]), "+v"(f[5]), "+v"(f[6]), "+v"(f[7]) :: "memory");
        unsigned np = 0;
#pragma unroll
        for (int i = 0; i < 8; i++) np |= __any(max(max(f[i][0], f[i][1]), max(f[i][2], f[i][3])) == ~0u) ? (1u << i) : 0u;
        pend = __builtin_amdgcn_readfirstlane(np);
        if (pend == 0) break;
    }
}

#define SLP 4
#define LD7 "s_nop 4\n\tbuffer_load_dwordx4 %0, %7, %8, %9 offen sc1\n\tbuffer_load_dwordx4 %1, %7, %8, %9 offen offset:1024 sc1\n\t"   \
            "buffer_load_dwordx4 %2, %7, %8, %9 offen offset:2048 sc1\n\tbuffer_load_dwordx4 %3, %7, %8, %9 offen offset:3072 sc1\n\t"    \
            "buffer_load_dwordx4 %4, %7, %8, %10 offen sc1\n\tbuffer_load_dwordx4 %5, %7, %8, %10 offen offset:1024 sc1\n\t"              \
            "buffer_load_dwordx4 %6, %7, %8, %10 offen offset:2048 sc1"
DEVI void issue7(u32x4 (&f)[7], rsrc_t rw, unsigned voff, unsigned soff) {
    asm volatile(LD7 : "=&v"(f[0]), "=&v"(f[1]), "=&v"(f[2]), "=&v"(f[3]), "=&v"(f[4]), "=&v"(f[5]), "=&v"(f[6])
                 : "v"(voff), "s"(rw), "s"(soff), "s"(soff + 4096) : "memory");
}
DEVI void issue7if(u32x4 (&f)[7], unsigned cond, rsrc_t rw, unsigned voff, unsigned soff) {
    asm volatile("s_cmp_eq_u32 %11, 0\n\ts_cbranch_scc1 .Lno7_%=\n\t" LD7 "\n.Lno7_%=:"
                 : "+v"(f[0]), "+v"(f[1]), "+v"(f[2]), "+v"(f[3]), "+v"(f[4]), "+v"(f[5]), "+v"(f[6])
                 : "v"(voff), "s"(rw), "s"(soff), "s"(soff + 4096), "s"(cond) : "memory", "scc");
}
DEVI void probe(rsrc_t rw, unsigned pv, unsigned soff) {
#pragma unroll 1
    for (unsigned spins = 0; spins < (1u << 18); spins++) {
        unsigned m;
        asm volatile("s_nop 4\n\tbuffer_load_dword %0, %1, %2, %3 offen sc1\n\ts_waitcnt vmcnt(0)" : "=&v"(m) : "v"(pv), "s"(rw), "s"(soff) : "memory");
        if (!__any(m == ~0u)) break;
        __builtin_amdgcn_s_sleep(SLP);
    }
}
template <int N> DEVI void check7(u32x4 (&f)[7], rsrc_t rw, unsigned voff, unsigned soff) {
    asm volatile("s_waitcnt vmcnt(%7)" : "+v"(f[0]), "+v"(f[1]), "+v"(f[2]), "+v"(f[3]), "+v"(f[4]), "+v"(f[5]), "+v"(f[6]) : "n"(N) : "memory");
#pragma unroll 1
    for (unsigned spins = 0; spins < 64; spins++) {
        unsigned m = 0;
#pragma unroll
        for (int i = 0; i < 7; i++) m = max(max(m, max(f[i][0], f[i][1])), max(f[i][2], f[i][3]));
        if (!__any(m == ~0u)) break;
        probe(rw, min(voff, 96u) * 64, soff);
        asm volatile(LD7 "\n\ts_waitcnt vmcnt(0)" : "+v"(f[0]), "+v"(f[1]), "+v"(f[2]), "+v"(f[3]), "+v"(f[4]), "+v"(f[5]), "+v"(f[6])
                     : "v"(voff), "s"(rw), "s"(soff), "s"(soff + 4096) : "memory");
    }
}

__global__ __launch_bounds__(512, 2) void k2_kernel(const unsigned short* __restrict__ x, const unsigned short* __restrict__ b1,
                                                    unsigned short* __restrict__ out, char* ws) {
    __shared__ __align__(16) char smem[L2_END];
    const unsigned sb = (unsigned)(size_t)(LDS3 char*)smem;
    const int tid = threadIdx.x, lane = tid & 63, w = __builtin_amdgcn_readfirstlane(tid >> 6);
    const int b = blockIdx.x, g = (b & 7) * 4 + (b >> 6), j = (b >> 3) & 7;
    const int r = lane & 31, h = lane >> 5, kh = w >> 2;
    const unsigned l16 = lane * 16;
    const rsrc_t rw = mkrsrc(ws, WS_END);
    STAMPDECL STAMP(0);
    u32x4 q0, q1; u32x2 km;
    asm volatile("s_nop 4\n\tbuffer_load_dwordx4 %0, %3, %4, %5 offen\n\tbuffer_load_dwordx4 %1, %3, %4, %6 offen\n\tbuffer_load_dwordx2 %2, %7, %4, 0 offen"
                 : "=&v"(q0), "=&v"(q1), "=&v"(km)
                 : "v"(l16), "s"(rw), "s"(Q_OFF + ((g * 2) * 32 + 4 * j + (w & 3)) * 1024), "s"(Q_OFF + ((g * 2 + 1) * 32 + 4 * j + (w & 3)) * 1024),
                   "v"(KMAX_OFF + lane * 8) : "memory");
#define KVCHUNK(off, n) _Pragma("unroll") for (int i = 0; i < n; i++) { const unsigned o_ = kh * 65536 + (off) + ((w & 3) * n + i) * 1024; dma16(rw, l16, KV_OFF + o_, sb + o_); }
#define WOPIECE(i) dma16(rw, l16, WO_OFF + (j * 64 + w * 8 + (i)) * 1024, sb + kh * 65536 + (w & 3) * 8192 + (i) * 1024);
    KVCHUNK(0, 2)
    BAR();
    KVCHUNK(8192, 4)
    if (tid < 256) sts128(sb + L2_ONES + tid * 16, u32x4{0x3f803f80u, 0x3f803f80u, 0x3f803f80u, 0x3f803f80u});
    asm volatile("s_waitcnt vmcnt(6)" : "+v"(q0), "+v"(q1), "+v"(km) :: "memory");
    float qn0 = 0.f, qn1 = 0.f;
#pragma unroll
    for (int e = 0; e < 4; e++) { qn0 = dot2(q0[e], q0[e], qn0); qn1 = dot2(q1[e], q1[e], qn1); }
    const float kmax2 = wavemax(fmaxf(__builtin_bit_cast(float, km[0]), __builtin_bit_cast(float, km[1])));
    const float mb = __builtin_sqrtf(fmaxf(xsum32(qn0), xsum32(qn1)) * kmax2) * 1.001f;
    const bool fast = __all(mb <= 60.f);
    const unsigned aslot = A_OFF + ((g * 8 + j) * 28 + 2 * (w < 7 ? w : 6)) * 1024;
    const rsrc_t ra = mkrsrc(ws, w < 7 ? WS_END : 0);
#define STA(v, soff) stc(v, ra, l16, soff)
#define MARK(i) if ((i) < 4) STA((u32x4{~0u, ~0u, ~0u, ~0u}), aslot + (((i) >> 1) * 14 + ((i) & 1)) * 1024); else STC((u32x4{~0u, ~0u, ~0u, ~0u}), l16, hslot);
    const int tt = w & 1, ct = (w >> 1) & 1;
    const unsigned hslot = H_OFF + (g * 64 + 8 * j + 4 * tt + 2 * ct + kh) * 1024;
    const unsigned w1o = W1_OFF + ((7 * j + (w < 7 ? w : 6)) * 32) * 1024;
    u32x4 w1f[32];
    f32x16 o0, o1;
    float m0, m1;
#pragma unroll
    for (int i = 0; i < 16; i++) { o0[i] = 0.f; o1[i] = 0.f; }
    unsigned ka = sb + kh * 65536 + l16, va = r < 16 ? sb + kh * 65536 + 1024 + h * 256 + r * 16 : sb + L2_ONES + ((r >= 20 && r < 28) ? 64 : 0);
    const unsigned vinc = r < 16 ? 2048 : 0;
    u32x4 warm = {0, 0, 0, 0};
#define TOUCH(i) asm volatile("s_nop 4\n\tbuffer_load_dwordx4 %0, %1, %2, %3 offen" : "+v"(warm) : "v"(l16), "s"(rw),                  \
        "s"((i) < 2 ? w1o + (24 + (b >> 6) * 2 + (i)) * 1024 : W2_OFF + (j * 224 + w * 28 + (b >> 6) * 7 + (i) - 2) * 1024) : "memory");
    if (fast) {
        f32x16 nm, sA, sB;
#pragma unroll
        for (int i = 0; i < 16; i++) nm[i] = -mb;
        m0 = mb; m1 = mb;
        u32x4 p1 = {0, 0, 0, 0}, k0, k1, va0, va1, vb0, vb1;
#define EY(S, i) e[i] = __builtin_amdgcn_exp2f(S[i]); SB;
#define CV(P, k, i) P[k] = cvtpk(e[i], e[i + 1]); SB;
#define STEP(S_, OP, VP1, SN, KN, QN, OC, VC0, LD) { float e[16]; u32x4 p0;                                                   \
        EY(S_, 0) EY(S_, 1) OP = MF32(B8(VP1), B8(p1), OP); SB; EY(S_, 2) EY(S_, 3) CV(p0, 0, 0) EY(S_, 4) EY(S_, 5) CV(p0, 1, 2) EY(S_, 6) \
        SN = MF32(B8(KN), B8(QN), nm); SB; LD EY(S_, 7) CV(p0, 2, 4) EY(S_, 8) EY(S_, 9) CV(p0, 3, 6) EY(S_, 10) EY(S_, 11)       \
        OC = MF32(B8(VC0), B8(p0), OC); SB; CV(p1, 0, 8) EY(S_, 12) EY(S_, 13) CV(p1, 1, 10) EY(S_, 14) EY(S_, 15) CV(p1, 2, 12) CV(p1, 3, 14) }
#define LDKV(K_, V0_, V1_, T) K_ = lds128(ka + (T) * 2048); V0_ = lds128(va); V1_ = lds128(va + 512); va += vinc; asm volatile("" : "+v"(va)); SB;
        WAITV(4); WAITL(); BAR();
        STAMP(11);
        LDKV(k0, va0, va1, 0)
        vb1 = va1;
        sA = MF32(B8(k0), B8(q0), nm);
#pragma unroll
        for (int it = 0; it < 16; it++) {
            if (it == 1) { WAITV(5); BAR(); }
            if (it == 5) { WAITV(14); BAR(); }
            if (it == 10) { WAITV(26); BAR(); }
            SB;
            STEP(sA, o1, vb1, sB, k0, q1, o0, va0, LDKV(k1, vb0, vb1, 2 * it + 1))
            if (it == 0) { KVCHUNK(24576, 5) } if (it == 1) { KVCHUNK(45056, 5) }
            if (it >= 2 && it < 14) { w1f[2 * it - 4] = LDW(l16, w1o + (2 * it - 4) * 1024); SB; }
            STEP(sB, o0, va1, sA, k1, q0, o1, va0, )
            if (it >= 2 && it < 7) { MARK(it - 2) SB; }
            if (it >= 10 && it < 14) { WOPIECE(2 * it - 20) WOPIECE(2 * it - 19) }
            STEP(sA, o1, va1, sB, k1, q1, o0, vb0, LDKV(k0, va0, va1, 2 * it + 2))
            if (it >= 2 && it < 14) { w1f[2 * it - 3] = LDW(l16, w1o + (2 * it - 3) * 1024); SB; }
            STEP(sB, o0, vb1, sA, k0, q0, o1, vb0, )
            if (it >= 5 && it < 14) { TOUCH(it - 5) SB; }
        }
        o1 = MF32(B8(vb1), B8(p1), o1);
    } else {
        KVCHUNK(24576, 5) KVCHUNK(45056, 5)
        WAITV(0); WAITL(); BAR();
        m0 = -3.0e38f; m1 = -3.0e38f;
        const f32x16 z = {0, 0, 0, 0, 0, 0, 0, 0, 0, 0, 0, 0, 0, 0, 0, 0};
#define SLOW(Q_, O_, M_) { f32x16 s = MF32(B8(kf), B8(Q_), z); float mx = s[0];                                               \
            _Pragma("unroll") for (int i = 1; i < 16; i++) mx = fmaxf(mx, s[i]);                                              \
            mx = fmaxf(xmax32(mx), M_); const float al = __builtin_amdgcn_exp2f(M_ - mx); M_ = mx; u32x4 p0, p1;              \
            _Pragma("unroll") for (int i = 0; i < 16; i++) O_[i] *= al;                                                       \
            _Pragma("unroll") for (int i = 0; i < 4; i++) {                                                                   \
                p0[i] = cvtpk(__builtin_amdgcn_exp2f(s[2 * i] - mx), __builtin_amdgcn_exp2f(s[2 * i + 1] - mx));              \
                p1[i] = cvtpk(__builtin_amdgcn_exp2f(s[2 * i + 8] - mx), __builtin_amdgcn_exp2f(s[2 * i + 9] - mx)); }        \
            O_ = MF32(B8(v0), B8(p0), O_); O_ = MF32(B8(v1), B8(p1), O_); }
#pragma unroll 1
        for (int t = 0; t < 32; t++) {
            if (t == 2 || t == 10 || t == 20) { WAITL(); BAR(); }
            if (t == 20) { _Pragma("unroll") for (int i = 0; i < 8; i++) WOPIECE(i) }
            const u32x4 kf = lds128(ka), v0 = lds128(va), v1 = lds128(va + 512);
            ka += 2048; va += vinc;
            SLOW(q0, o0, m0) SLOW(q1, o1, m1)
        }
#pragma unroll
        for (int i = 0; i < 24; i++) w1f[i] = LDW(l16, w1o + i * 1024);
#pragma unroll
        for (int i = 0; i < 5; i++) { MARK(i) }
    }
    STAMP(1);
    const f32x16 z16 = {0, 0, 0, 0, 0, 0, 0, 0, 0, 0, 0, 0, 0, 0, 0, 0};
    f32x16 cs = z16;
#define COLSUM(k0, k1) _Pragma("unroll") for (int ks = k0; ks < k1; ks++) cs = MF32(B8(w1f[ks]), B8((u32x4{0x3f803f80u, 0x3f803f80u, 0x3f803f80u, 0x3f803f80u})), cs); \
    asm volatile("" : "+v"(cs));
    {
        const f32x16 os = kh ? o0 : o1, ok = kh ? o1 : o0;
        const float ms = kh ? m0 : m1, mk = kh ? m1 : m0;
        const unsigned xa = sb + L2_X + w * 2560, xb = sb + L2_X + (w ^ 4) * 2560;
        *(LDS3 f32x4*)(size_t)(xa + l16) = f32x4{os[0], os[1], os[2], os[3]};
        *(LDS3 f32x4*)(size_t)(xa + 1024 + l16) = f32x4{os[4], os[5], os[6], os[7]};
        *(LDS3 f32x2*)(size_t)(xa + 2048 + lane * 8) = f32x2{os[8], ms};
        WAITV(0); WAITL(); BAR();
        asm volatile("" :: "v"(warm));
        const f32x4 a = *(LDS3 f32x4*)(size_t)(xb + l16), c = *(LDS3 f32x4*)(size_t)(xb + 1024 + l16);
        const f32x2 lm = *(LDS3 f32x2*)(size_t)(xb + 2048 + lane * 8);
        const float mm = fmaxf(mk, lm[1]), ea = __builtin_amdgcn_exp2f(mk - mm), eb = __builtin_amdgcn_exp2f(lm[1] - mm);
        const float inv = 1.f / (ok[8] * ea + lm[0] * eb), fa = ea * inv, fb = eb * inv;
        u32x4 of;
        of[0] = cvtpk(ok[0] * fa + a[0] * fb, ok[1] * fa + a[1] * fb); of[1] = cvtpk(ok[2] * fa + a[2] * fb, ok[3] * fa + a[3] * fb);
        of[2] = cvtpk(ok[4] * fa + c[0] * fb, ok[5] * fa + c[1] * fb); of[3] = cvtpk(ok[6] * fa + c[2] * fb, ok[7] * fa + c[3] * fb);
        STC(clean(of), l16, O_OFF + ((g * 2 + kh) * 32 + 4 * j + (w & 3)) * 1024);
    }
    STAMP(2);
    const unsigned xo = (64 * g + 32 * tt + r) * 512 + 64 * j + 32 * ct + 16 * kh;
    const u32x2 xa0 = *(const u32x2*)(x + xo + 4 * h), xa1 = *(const u32x2*)(x + xo + 8 + 4 * h);
    u32x2 bb[4];
#pragma unroll
    for (int i = 0; i < 4; i++) bb[i] = *(const u32x2*)(b1 + 224 * j + 32 * (w < 7 ? w : 6) + 8 * i + 4 * h);
    const unsigned myr = sb + kh * 65536 + 32768 + (w & 3) * 8192;
    const unsigned w2o = W2_OFF + (j * 224 + w * 28) * 1024;
    float hv[8];
    SB; COLSUM(0, 24) SB;
    {
        const int kq = w >> 1;
        u32x4 of[8], af[4];
        const unsigned wa = sb + kq * 8192 + l16;
        af[0] = lds128(wa); af[1] = lds128(wa + 65536); af[2] = lds128(wa + 1024); af[3] = lds128(wa + 65536 + 1024);
        poll8(of, rw, l16, O_OFF + ((g * 2 + tt) * 32 + 8 * kq) * 1024, 1024);
        u32x2 xb0 = xa0, xb1 = xa1;
        asm volatile("" : "+v"(xb0), "+v"(xb1), "+v"(bb[0]), "+v"(bb[1]), "+v"(bb[2]), "+v"(bb[3]));
        STAMP(3);
        f32x16 acc[2] = {z16, z16};
#pragma unroll
        for (int i = 0; i < 16; i++) {
            acc[i & 1] = MF32(B8(af[i & 3]), B8(of[i >> 1]), acc[i & 1]); SB;
            if (i < 12) { af[i & 3] = lds128(wa + (i & 1) * 65536 + ((i >> 1) + 2) * 1024); SB; }
            if (i & 1) { w1f[24 + (i >> 1)] = LDW(l16, w1o + (24 + (i >> 1)) * 1024); SB; }
        }
#pragma unroll
        for (int p = 0; p < 4; p++) {
            const unsigned xa = sb + (p >> 1) * 65536 + 32768 + (tt + 2 * (p & 1)) * 8192 + kq * 2048 + l16, o = (p >> 1) * 8;
            const f32x16 v = acc[p & 1];
            *(LDS3 f32x4*)(size_t)xa = f32x4{v[o], v[o + 1], v[o + 2], v[o + 3]};
            *(LDS3 f32x4*)(size_t)(xa + 1024) = f32x4{v[o + 4], v[o + 5], v[o + 6], v[o + 7]};
        }
        WAITL(); BAR();
        f32x4 a = {blo(xb0[0]), bhi(xb0[0]), blo(xb0[1]), bhi(xb0[1])}, c = {blo(xb1[0]), bhi(xb1[0]), blo(xb1[1]), bhi(xb1[1])};
#pragma unroll
        for (int p = 0; p < 4; p++) { a += *(LDS3 f32x4*)(size_t)(myr + p * 2048 + l16); c += *(LDS3 f32x4*)(size_t)(myr + p * 2048 + 1024 + l16); }
#pragma unroll
        for (int i = 0; i < 4; i++) { hv[i] = a[i]; hv[4 + i] = c[i]; }
        STC(clean(u32x4{cvtpk(hv[0], hv[1]), cvtpk(hv[2], hv[3]), cvtpk(hv[4], hv[5]), cvtpk(hv[6], hv[7])}), l16, hslot);
    }
    STAMP(4);
    SB; COLSUM(24, 32) SB;
#pragma unroll
    for (int i = 0; i < 4; i++) dma16(rw, l16, w2o + i * 1024, i < 8 ? myr + i * 1024 : sb + L2_W2 + (w * 3 + i - 8) * 1024);
    {
        u32x4 hf[8];
        poll8(hf, rw, l16, H_OFF + (g * 64 + 8 * w) * 1024, 1024);
        STAMP(5);
#pragma unroll
        for (int i = 0; i < 8; i++) sts128(sb + (i >> 2) * 65536 + (4 * w + (i & 3)) * 1024 + l16, hf[i]);
        f32x4 st;
#pragma unroll
        for (int t = 0; t < 2; t++) {
            float s0 = 0.f, q0_ = 0.f, s1 = 0.f, q1_ = 0.f;
#pragma unroll
            for (int i = 0; i < 4; i++)
#pragma unroll
                for (int e = 0; e < 4; e += 2) {
                    s0 = dot2(hf[4 * t + i][e], 0x3f803f80u, s0); q0_ = dot2(hf[4 * t + i][e], hf[4 * t + i][e], q0_);
                    s1 = dot2(hf[4 * t + i][e + 1], 0x3f803f80u, s1); q1_ = dot2(hf[4 * t + i][e + 1], hf[4 * t + i][e + 1], q1_);
                }
            st[2 * t] = xsum32(s0 + s1); st[2 * t + 1] = xsum32(q0_ + q1_);
        }
        if (h == 0) *(LDS3 f32x4*)(size_t)(sb + L2_STAT + w * 512 + r * 16) = st;
        if (tid == 0) *(LDS3 unsigned*)(size_t)(sb + L2_ONES) = 0;
        WAITL(); BAR();
    }
    STAMP(6);
    __builtin_amdgcn_s_setprio(1);
    u32x4 w2f[17], fa[7], fb[7];
    const unsigned ab = A_OFF + (g * 8 + w) * 28 * 1024;
    {
        f32x16 ac[2] = {z16, z16};
        u32x4 bf[4];
        unsigned ha0 = sb + l16, ha1 = sb + 65536 + l16;
        asm volatile("" : "+v"(ha1));
#pragma unroll
        for (int i = 0; i < 4; i++) bf[i] = lds128(((i & 1) ? ha1 : ha0) + (i >> 1) * 1024);
        f32x4 st = {0, 0, 0, 0};
        float mean[2], rstd[2];
        SB;
        sfor<64>([&](auto K) __attribute__((always_inline)) {
            constexpr int n = K.v;
            ac[n & 1] = MF32(B8(w1f[n >> 1]), B8(bf[n & 3]), ac[n & 1]); SB;
            if (n < 60) { bf[n & 3] = lds128(((n & 1) ? ha1 : ha0) + ((n >> 1) + 2) * 1024); SB; }
            if (n >= 4 && n < 12) { st += *(LDS3 f32x4*)(size_t)(sb + L2_STAT + (n - 4) * 512 + r * 16); SB; }
            if (n == 16) {
#pragma unroll
                for (int t = 0; t < 2; t++) {
                    mean[t] = st[2 * t] * (1.f / 512.f);
                    rstd[t] = __builtin_amdgcn_rsqf(fmaxf(st[2 * t + 1] * (1.f / 512.f) - mean[t] * mean[t], 0.f) + 1e-5f);
                }
                SB;
            }
            constexpr int m = (n >> 1) + 4;
            if ((n & 1) && m < 11) { dma16(rw, l16, w2o + m * 1024, m < 8 ? myr + m * 1024 : sb + L2_W2 + (w * 3 + m - 8) * 1024); SB; }
            if ((n & 1) && m >= 11 && m < 28) { w2f[m >= 11 && m < 28 ? m - 11 : 0] = LDW(l16, w2o + m * 1024); SB; }
        });
        STAMP(12);
#define PIN(x) asm volatile("" : "+v"(x));
        sfor<4>([&](auto K) __attribute__((always_inline)) {
            constexpr int t = K.v >> 1, s = K.v & 1;
            float z[8], u[8];
            u32x4 yp;
#define EL8 _Pragma("unroll") for (int i = 0; i < 8; i++)
            EL8 { u[i] = fmaf(-mean[t], cs[8 * s + i], ac[t][8 * s + i]); PIN(u[i]) }
            EL8 { const unsigned bw = bb[2 * s + (i >> 2)][(i >> 1) & 1]; z[i] = (i & 1) ? bhi(bw) : blo(bw); PIN(z[i]) }
            EL8 { z[i] = fmaf(u[i], rstd[t], z[i]); PIN(z[i]) }
            EL8 { u[i] = z[i] * LOG2E; PIN(u[i]) }
            EL8 { u[i] = __builtin_amdgcn_exp2f(u[i]); PIN(u[i]) }
            float a_[8];
            EL8 { a_[i] = u[i] + 2.f; PIN(a_[i]) }
            EL8 { u[i] = fmaf(u[i], a_[i], 2.f); PIN(u[i]) }
            EL8 { u[i] = __builtin_amdgcn_rcpf(u[i]); PIN(u[i]) }
            EL8 { u[i] = fmaf(u[i], -2.f, 1.f); PIN(u[i]) }
            EL8 { z[i] *= u[i]; PIN(z[i]) }
            EL8 if (i < 4) { yp[i & 3] = cvtpk(z[2 * (i & 3)], z[2 * (i & 3) + 1]); PIN(yp[i & 3]) }
            STA(clean(yp), aslot + (t * 14 + s) * 1024); SB;
            if (K.v == 1) {
                asm volatile("" : "+v"(w2f[0]), "+v"(w2f[1]), "+v"(w2f[2]), "+v"(w2f[3]), "+v"(w2f[4]), "+v"(w2f[5]), "+v"(w2f[6]), "+v"(w2f[7]), "+v"(w2f[8]),
                                  "+v"(w2f[9]), "+v"(w2f[10]), "+v"(w2f[11]), "+v"(w2f[12]), "+v"(w2f[13]), "+v"(w2f[14]), "+v"(w2f[15]), "+v"(w2f[16]));
#pragma unroll
                for (int i = 0; i < 7; i++) asm volatile("" : "=v"(fa[i]), "=v"(fb[i]));
                issue7if(fa, kh, rw, l16, ab); issue7if(fb, kh, rw, l16, ab + 14336); SB;
                STAMP(13);
            }
        });
    }
    STAMP(7);
    {
        u32x4 wl[2];
        __builtin_amdgcn_s_setprio(0);
        LDS3 unsigned* done = (LDS3 unsigned*)(size_t)(sb + L2_ONES);
        if (lane == 0) __hip_atomic_fetch_add(done, 1u, __ATOMIC_RELAXED, __HIP_MEMORY_SCOPE_WORKGROUP);
#define ALLDONE while (__builtin_amdgcn_readfirstlane(__hip_atomic_load(done, __ATOMIC_RELAXED, __HIP_MEMORY_SCOPE_WORKGROUP)) < 8) __builtin_amdgcn_s_sleep(2);
        const unsigned pv = (lane < 7 ? lane : lane < 14 ? lane + 7 : 0) * 1024;
        issue7if(fa, kh ^ 1, rw, l16, ab);
        if (!kh) probe(rw, min(l16, 96u) * 64, ab + 14336);
        issue7if(fb, kh ^ 1, rw, l16, ab + 14336);
        unsigned wla = myr + l16, wlb = sb + L2_W2 + w * 3072 + l16;
        asm volatile("" : "+v"(wla), "+v"(wlb));
#define WLD(n) lds128((n) < 8 ? wla + (n) * 1024 : wlb + ((n) - 8) * 1024)
#define F2(F, ks0, C0, C1) _Pragma("unroll") for (int k = 0; k < 7; k++) { const int n0 = 2 * ((ks0) + k), n1 = n0 + 1;               \
            C0 = MF32(B8(n0 < 11 ? wl[0] : w2f[n0 < 11 ? 0 : n0 - 11]), B8(F[k]), C0); SB; if (n0 + 2 < 11) { wl[0] = WLD(n0 + 2); SB; } \
            C1 = MF32(B8(n1 < 11 ? wl[1] : w2f[n1 < 11 ? 0 : n1 - 11]), B8(F[k]), C1); SB; if (n1 + 2 < 11) { wl[1] = WLD(n1 + 2); SB; } }
#define PST(v, t, cc, s) sts128(sb + (t) * 65536 + ((cc) + 2 * (s)) * 8192 + w * 1024 + l16,                                          \
            u32x4{cvtpk(v[8 * (s)], v[8 * (s) + 1]), cvtpk(v[8 * (s) + 2], v[8 * (s) + 3]), cvtpk(v[8 * (s) + 4], v[8 * (s) + 5]), cvtpk(v[8 * (s) + 6], v[8 * (s) + 7])});
        f32x16 c0 = z16, c1 = z16, c2 = z16, c3 = z16;
        wl[0] = WLD(0); wl[1] = WLD(1);
        check7<7>(fa, rw, l16, ab);
        STAMP(14);
        F2(fa, 0, c0, c1)
        issue7if(fa, kh, rw, l16, ab + 7168);
        wl[0] = WLD(0); wl[1] = WLD(1);
        if (!kh) WAITV(0);
        check7<7>(fb, rw, l16, ab + 14336);
        F2(fb, 0, c2, c3)
        if (!kh) { ALLDONE probe(rw, pv, ab + 7168); }
        issue7if(fa, kh ^ 1, rw, l16, ab + 7168);
        issue7(fb, rw, l16, ab + 21504);
        check7<7>(fa, rw, l16, ab + 7168);
        F2(fa, 7, c0, c1)
        if (kh) { ALLDONE asm volatile("" ::: "memory"); }
        PST(c0, 0, 0, 0) PST(c0, 0, 0, 1) PST(c1, 0, 1, 0) PST(c1, 0, 1, 1)
        check7<0>(fb, rw, l16, ab + 21504);
        STAMP(15);
        F2(fb, 7, c2, c3)
        STAMP(8);
        PST(c2, 1, 0, 0) PST(c2, 1, 0, 1) PST(c3, 1, 1, 0) PST(c3, 1, 1, 1)
        WAITL(); BAR();
        STAMP(9);
        unsigned sl = 0x00003f80u, sh = 0x3f800000u;
        asm volatile("" : "+s"(sl), "+s"(sh));
#pragma unroll
        for (int i = 0; i < 8; i++) {
            const u32x4 pf = lds128(sb + tt * 65536 + (w >> 1) * 8192 + i * 1024 + l16);
#pragma unroll
            for (int e = 0; e < 4; e++) { hv[2 * e] = dot2(pf[e], sl, hv[2 * e]); hv[2 * e + 1] = dot2(pf[e], sh, hv[2 * e + 1]); }
        }
        const u32x2 s0 = __builtin_amdgcn_permlane32_swap(cvtpk(hv[0], hv[1]), cvtpk(hv[4], hv[5]), false, false);
        const u32x2 s1 = __builtin_amdgcn_permlane32_swap(cvtpk(hv[2], hv[3]), cvtpk(hv[6], hv[7]), false, false);
        *(u32x4*)(out + xo + 8 * h) = u32x4{s0[0], s1[0], s0[1], s1[1]};
    }
    STAMP(10);
}

extern "C" void kernel_launch(void* const* d_in, const int* in_sizes, int n_in, void* d_out, int out_size, void* d_ws, size_t ws_size,
                              hipStream_t stream) {
    typedef const unsigned short* P;
    k1_kernel<<<256, 512, 0, stream>>>((P)d_in[0], (P)d_in[1], (P)d_in[2], (P)d_in[3], (P)d_in[4], (P)d_in[6], (P)d_in[8], (char*)d_ws);
    k2_kernel<<<256, 512, 0, stream>>>((P)d_in[0], (P)d_in[7], (unsigned short*)d_out, (char*)d_ws);
}
```

```cpp
#include <hip/hip_runtime.h>
#include <hip/hip_bf16.h>

typedef __attribute__((ext_vector_type(8))) __bf16 bf16x8;
typedef __attribute__((ext_vector_type(4))) __bf16 bf16x4;
typedef __attribute__((ext_vector_type(2))) __bf16 bf16x2;
typedef __attribute__((ext_vector_type(16))) float f32x16;
typedef __attribute__((ext_vector_type(4))) float f32x4;
typedef __attribute__((ext_vector_type(2))) float f32x2;
typedef __attribute__((ext_vector_type(4))) unsigned u32x4;
typedef __attribute__((ext_vector_type(2))) unsigned u32x2;
typedef __attribute__((ext_vector_type(4))) short s16x4;
typedef __amdgpu_buffer_rsrc_t rsrc_t;
#define LDS3 __attribute__((address_space(3)))
#define GLB1 __attribute__((address_space(1)))
#define DEVI __device__ __forceinline__
#define SB __builtin_amdgcn_sched_barrier(0)
#define B8(x) __builtin_bit_cast(bf16x8, x)
#define MF32(a, b, c) __builtin_amdgcn_mfma_f32_32x32x16_bf16(a, b, c, 0, 0, 0)
#define MF16(a, b, c) __builtin_amdgcn_mfma_f32_16x16x32_bf16(a, b, c, 0, 0, 0)
#define STAMP(i)
#define STAMP1(i)
#define STAMP1DECL
#define STAMPDECL

constexpr unsigned FLAG_OFF = 0, KMAX_OFF = 16384, KV_OFF = 32768, Q_OFF = 0x40000, WO_OFF = Q_OFF + (2u << 20),
                   W1_OFF = WO_OFF + (512u << 10), W2_OFF = W1_OFF + (1792u << 10), O_OFF = W2_OFF + (1792u << 10),
                   H_OFF = O_OFF + (2u << 20), A_OFF = H_OFF + (2u << 20), WS_END = A_OFF + (7u << 20);
constexpr unsigned K2_WARM = 28;
constexpr float LOG2E = 1.4426950408889634f;

template <int V> struct IC { static constexpr int v = V; };
template <int N, class F> DEVI void sfor(F&& f) { if constexpr (N > 0) { sfor<N - 1>(f); f(IC<N - 1>{}); } }
DEVI unsigned cvtpk(float a, float b) { f32x2 v = {a, b}; return __builtin_bit_cast(unsigned, __builtin_convertvector(v, bf16x2)); }
DEVI float blo(unsigned u) { return __builtin_bit_cast(float, u << 16); }
DEVI float bhi(unsigned u) { return __builtin_bit_cast(float, u & 0xffff0000u); }
DEVI rsrc_t mkrsrc(const void* p, unsigned bytes) { return __builtin_amdgcn_make_buffer_rsrc((void*)p, 0, bytes, 0x00020000); }
DEVI void dma16(rsrc_t rs, unsigned voff, unsigned soff, unsigned lds) {
    unsigned keep;
    asm volatile("s_mov_b32 %0, m0\n\ts_mov_b32 m0, %1\n\ts_nop 4\n\tbuffer_load_dwordx4 %2, %3, %4 offen lds\n\ts_mov_b32 m0, %0"
                 : "=&s"(keep) : "s"(lds), "v"(voff), "s"(rs), "s"(soff) : "memory");
}
#define WAITV(n) asm volatile("s_waitcnt vmcnt(" #n ")" ::: "memory")
#define WAITL() asm volatile("s_waitcnt lgkmcnt(0)" ::: "memory")
#define BAR() asm volatile("s_barrier" ::: "memory")
DEVI u32x4 lds128(unsigned a) { return *(LDS3 u32x4*)(size_t)a; }
DEVI void sts128(unsigned a, u32x4 v) { *(LDS3 u32x4*)(size_t)a = v; }
DEVI f32x2 swp32(float x) { float y = x; asm("s_nop 1\n\tv_permlane32_swap_b32 %0, %1\n\ts_nop 1" : "+v"(x), "+v"(y)); return f32x2{x, y}; }
DEVI f32x2 swp16(float x) { float y = x; asm("s_nop 1\n\tv_permlane16_swap_b32 %0, %1\n\ts_nop 1" : "+v"(x), "+v"(y)); return f32x2{x, y}; }
DEVI float xsum32(float x) { const f32x2 t = swp32(x); return t[0] + t[1]; }
DEVI float xmax32(float x) { const f32x2 t = swp32(x); return fmaxf(t[0], t[1]); }
DEVI float xsum16(float x) { const f32x2 t = swp16(x); return t[0] + t[1]; }
DEVI float xmax16(float x) { const f32x2 t = swp16(x); return fmaxf(t[0], t[1]); }
template <int C> DEVI float dppf(float x) { return __builtin_bit_cast(float, __builtin_amdgcn_update_dpp(0, __builtin_bit_cast(int, x), C, 0xf, 0xf, true)); }
DEVI float rowsum16(float x) { x += dppf<0xB1>(x); x += dppf<0x4E>(x); x += dppf<0x141>(x); x += dppf<0x140>(x); return x; }
DEVI float rowmax16(float x) { x = fmaxf(x, dppf<0xB1>(x)); x = fmaxf(x, dppf<0x4E>(x)); x = fmaxf(x, dppf<0x141>(x)); x = fmaxf(x, dppf<0x140>(x)); return x; }
DEVI float wavesum(float x) { return xsum32(xsum16(rowsum16(x))); }
DEVI float wavemax(float x) { return xmax32(xmax16(rowmax16(x))); }
DEVI float dot2(unsigned a, unsigned b, float c) { return __builtin_amdgcn_fdot2_f32_bf16(__builtin_bit_cast(bf16x2, a), __builtin_bit_cast(bf16x2, b), c, false); }

#define LDW(voff, soff) __builtin_amdgcn_raw_buffer_load_b128(rw, voff, soff, 0)
#define LDC(voff, soff) __builtin_amdgcn_raw_buffer_load_b128(rw, voff, soff, 16)
DEVI void stc(u32x4 v, rsrc_t rs, unsigned voff, unsigned soff) { __builtin_amdgcn_raw_buffer_store_b128(v, rs, voff, soff, 16); asm volatile("s_nop 1" :: "v"(v) : "memory"); }
#define STC(v, voff, soff) stc(v, rw, voff, soff)
constexpr unsigned L1_X = 0, L1_WQ = 65536, L1_WKV = 131072, L1_PK = 147456, L1_END = 163840;
DEVI bf16x8 ldtr(unsigned a, unsigned b) {
    const s16x4 lo = __builtin_amdgcn_ds_read_tr16_b64_v4i16((LDS3 s16x4*)(size_t)a), hi = __builtin_amdgcn_ds_read_tr16_b64_v4i16((LDS3 s16x4*)(size_t)b);
    return B8(__builtin_shufflevector(lo, hi, 0, 1, 2, 3, 4, 5, 6, 7));
}
__global__ __launch_bounds__(512, 2) void k1_kernel(const unsigned short* __restrict__ x, const unsigned short* __restrict__ wq,
        const unsigned short* __restrict__ wk, const unsigned short* __restrict__ wv, const unsigned short* __restrict__ wo,
        const unsigned short* __restrict__ w1, const unsigned short* __restrict__ w2, char* __restrict__ ws) {
    __shared__ __align__(16) char smem[L1_END];
    const unsigned sb = (unsigned)(size_t)(LDS3 char*)smem;
    const int tid = threadIdx.x, lane = tid & 63, w = __builtin_amdgcn_readfirstlane(tid >> 6);
    const int b = blockIdx.x, g = (b & 7) * 4 + (b >> 6), j = (b >> 3) & 7;
    const int rl = lane >> 3, pl = lane & 7;
    const rsrc_t rx = mkrsrc(x, 2048 * 512 * 2), rq = mkrsrc(wq, 512 * 512 * 2), rkv = mkrsrc((j & 1) ? wv : wk, 512 * 16 * 2);
    STAMP1DECL STAMP1(0);
    u32x4 xr[8];
    {
        const unsigned vo = rl * 1024 + pl * 16, so = (g * 64 + w * 8) * 1024;
        asm volatile("s_nop 4\n\tbuffer_load_dwordx4 %0, %8, %9, %10 offen\n\tbuffer_load_dwordx4 %1, %8, %9, %10 offen offset:128\n\t"
                     "buffer_load_dwordx4 %2, %8, %9, %10 offen offset:256\n\tbuffer_load_dwordx4 %3, %8, %9, %10 offen offset:384\n\t"
                     "buffer_load_dwordx4 %4, %8, %9, %10 offen offset:512\n\tbuffer_load_dwordx4 %5, %8, %9, %10 offen offset:640\n\t"
                     "buffer_load_dwordx4 %6, %8, %9, %10 offen offset:768\n\tbuffer_load_dwordx4 %7, %8, %9, %10 offen offset:896"
                     : "=&v"(xr[0]), "=&v"(xr[1]), "=&v"(xr[2]), "=&v"(xr[3]), "=&v"(xr[4]), "=&v"(xr[5]), "=&v"(xr[6]), "=&v"(xr[7])
                     : "v"(vo), "s"(rx), "s"(so) : "memory");
    }
#pragma unroll
    for (int i = 0; i < 8; i++)
        dma16(rq, rl * 1024 + (((pl >> 1) ^ (((rl >> 1) & 1) | ((i & 1) << 1))) * 32) + (pl & 1) * 16, (64 * w + 8 * i) * 1024 + j * 128, sb + L1_WQ + (w * 8 + i) * 1024);
#pragma unroll
    for (int i = 0; i < 2; i++) dma16(rkv, lane * 16, (w * 2 + i) * 1024, sb + L1_WKV + (w * 2 + i) * 1024);
    asm volatile("s_waitcnt vmcnt(10)" : "+v"(xr[0]), "+v"(xr[1]), "+v"(xr[2]), "+v"(xr[3]), "+v"(xr[4]), "+v"(xr[5]), "+v"(xr[6]), "+v"(xr[7]) :: "memory");
    {
        float s0 = 0.f, s1 = 0.f, q0 = 0.f, q1 = 0.f;
#pragma unroll
        for (int i = 0; i < 8; i++)
#pragma unroll
            for (int e = 0; e < 4; e += 2) {
                s0 = dot2(xr[i][e], 0x3f803f80u, s0); q0 = dot2(xr[i][e], xr[i][e], q0);
                s1 = dot2(xr[i][e + 1], 0x3f803f80u, s1); q1 = dot2(xr[i][e + 1], xr[i][e + 1], q1);
            }
        float s = s0 + s1, q = q0 + q1;
        s += dppf<0xB1>(s); q += dppf<0xB1>(q); s += dppf<0x4E>(s); q += dppf<0x4E>(q); s += dppf<0x141>(s); q += dppf<0x141>(q);
        s *= (1.f / 512.f); q *= (1.f / 512.f);
        const float rstd = __builtin_amdgcn_rsqf(fmaxf(q - s * s, 0.f) + 1e-5f), nm = -s * rstd;
        const int row = w * 8 + rl;
#pragma unroll
        for (int i = 0; i < 8; i++) {
            u32x4 o;
#pragma unroll
            for (int e = 0; e < 4; e++) o[e] = cvtpk(fmaf(blo(xr[i][e]), rstd, nm), fmaf(bhi(xr[i][e]), rstd, nm));
            sts128(sb + L1_X + row * 1024 + (((8 * i + pl) ^ (row & 15)) * 16), o);
        }
    }
    unsigned pd0, pd1;
    {
        const int id = b * 8 + w;
        const unsigned short* src; int ld, k0, n0; unsigned bytes;
        if (id < 256)       { src = wo; ld = 512;  bytes = 512 * 512 * 2;  k0 = (id & 31) * 16; n0 = (id >> 5) * 64; pd0 = WO_OFF + ((id >> 5) * 64 + (id & 31)) * 1024; pd1 = pd0 + 32768; }
        else if (id < 1152) { const int i2 = id - 256; src = w1; ld = 1792; bytes = 512 * 1792 * 2; k0 = (i2 & 31) * 16; n0 = (i2 >> 5) * 64;
                              pd0 = W1_OFF + ((i2 >> 5) * 64 + (i2 & 31)) * 1024; pd1 = pd0 + 32768; }
        else                { const int i2 = id - 1152, jc = i2 / 112, fk = i2 - jc * 112;
                              src = w2; ld = 512; bytes = 512 * 1792 * 2; k0 = fk * 16; n0 = jc * 64; pd0 = W2_OFF + i2 * 2048; pd1 = pd0 + 1024; }
        const rsrc_t rp = mkrsrc(src, bytes);
#pragma unroll
        for (int i = 0; i < 2; i++) dma16(rp, rl * ld * 2 + pl * 16, ((k0 + 8 * i) * ld + n0) * 2, sb + L1_PK + w * 2048 + i * 1024);
    }
    WAITV(2); WAITL(); BAR();
    STAMP1(2);
    u32x4 warm;
    {
        unsigned long long pc;
        asm volatile("s_getpc_b64 vcc\n\ts_add_u32 vcc_lo, vcc_lo, _Z9k2_kernelPKtS0_PtPc@rel32@lo+4\n\t"
                     "s_addc_u32 vcc_hi, vcc_hi, _Z9k2_kernelPKtS0_PtPc@rel32@hi+12\n\ts_mov_b64 %0, vcc" : "=s"(pc) :: "vcc", "scc");
        const unsigned piece = ((b >> 3) * 8 + w) % K2_WARM;
        asm volatile("s_nop 4\n\tglobal_load_dwordx4 %0, %1, %2" : "=&v"(warm) : "v"(piece * 1024 + lane * 16), "s"(pc) : "memory");
    }
    const int c = lane >> 4, li = lane & 15, hq = w & 3, tt = w >> 2;
    const rsrc_t rw = mkrsrc(ws, WS_END);
    STC((u32x4{~0u, ~0u, ~0u, ~0u}), lane * 16, O_OFF + (b * 8 + w) * 1024);
    const unsigned arow = 8 * c + (li >> 2);
    const unsigned abase = sb + L1_WQ + arow * 128 + ((hq ^ (((li >> 3) & 1) | ((c & 1) << 1))) * 32) + (li & 3) * 8;
    unsigned bb[4];
#pragma unroll
    for (int m = 0; m < 4; m++) bb[m] = sb + L1_X + li * 1024 + (((4 * m + c) ^ li) * 16);
    const int sub = j >> 1;
    {
        const unsigned wb = sb + L1_WKV + arow * 32 + (li & 3) * 8 + w * 2048;
        const unsigned xb = sb + L1_X + (sub * 16 + li) * 1024 + (w >> 1) * 256;
        f32x4 d = {0, 0, 0, 0};
#pragma unroll
        for (int u = 0; u < 2; u++) {
            const bf16x8 wf = ldtr(wb + u * 1024, wb + u * 1024 + 128);
            const bf16x8 xf = B8(lds128(xb + (((8 * (w & 1) + 4 * u + c) ^ li) * 16)));
            if (j & 1) d = MF16(xf, wf, d); else d = MF16(wf, xf, d);
        }
        *(LDS3 f32x4*)(size_t)(sb + L1_WKV + w * 2048 + lane * 16) = d;
    }
    f32x4 a0 = {0, 0, 0, 0}, a1 = {0, 0, 0, 0};
    {
        bf16x8 af[3]; u32x4 b0[3], b1[3];
#define K1LD(ks) { af[(ks) % 3] = ldtr(abase + (ks) * 4096, abase + (ks) * 4096 + 512); const unsigned ba = bb[(ks) & 3] + tt * 32768 + ((ks) >> 2) * 256; \
                   b0[(ks) % 3] = lds128(ba); b1[(ks) % 3] = lds128(ba + 16384); SB; }
        K1LD(0) K1LD(1) K1LD(2)
#pragma unroll
        for (int ks = 0; ks < 16; ks++) {
            a0 = MF16(af[ks % 3], B8(b0[ks % 3]), a0); a1 = MF16(af[ks % 3], B8(b1[ks % 3]), a1); SB;
            if (ks < 13) K1LD(ks + 3)
        }
    }
    {
        const float sc = 0.25f * LOG2E;
        const u32x2 s0 = __builtin_amdgcn_permlane32_swap(cvtpk(a0[0] * sc, a0[1] * sc), cvtpk(a1[0] * sc, a1[1] * sc), false, false);
        const u32x2 s1 = __builtin_amdgcn_permlane32_swap(cvtpk(a0[2] * sc, a0[3] * sc), cvtpk(a1[2] * sc, a1[3] * sc), false, false);
        STAMP1(1);
        STC((u32x4{s0[0], s1[0], s0[1], s1[1]}), ((c & 1) * 32 + (c >> 1) * 16 + li) * 16, Q_OFF + ((g * 2 + tt) * 32 + 4 * j + hq) * 1024);
        WAITV(2);
        asm volatile("" :: "v"(warm));
        const unsigned pa = sb + L1_PK + w * 2048 + (4 * (c >> 1) + (li >> 2)) * 128 + (c & 1) * 32 + (li & 3) * 8;
        STC(__builtin_bit_cast(u32x4, ldtr(pa, pa + 1024)), lane * 16, pd0);
        STC(__builtin_bit_cast(u32x4, ldtr(pa + 64, pa + 1088)), lane * 16, pd1);
    }
    WAITL(); BAR();
    if (w == 0) {
        f32x4 d = {0, 0, 0, 0};
#pragma unroll
        for (int i = 0; i < 8; i++) d += *(LDS3 f32x4*)(size_t)(sb + L1_WKV + i * 2048 + lane * 16);
        const u32x2 o = {cvtpk(d[0], d[1]), cvtpk(d[2], d[3])};
        const int s = sub & 1;
        char* kv = ws + KV_OFF + (g * 2 + (sub >> 1)) * 2048;
        if (j & 1) *(u32x2*)(kv + 1024 + s * 512 + (c & 1) * 256 + li * 16 + (c >> 1) * 8) = o;
        else {
            *(u32x2*)(kv + ((c & 1) * 32 + 16 * s + li) * 16 + (c >> 1) * 8) = o;
            const float n2 = rowmax16(xsum32(xsum16(dot2(o[0], o[0], dot2(o[1], o[1], 0.f)))));
            if (lane == 0) ((float*)(ws + KMAX_OFF))[g * 4 + sub] = n2;
        }
    }
    STAMP1(3);
}

constexpr unsigned L2_STAT = 131072, L2_ONES = 135168, L2_X = 139264, L2_W2 = 139264, L2_END = 163840;
DEVI u32x4 clean(u32x4 v) { return u32x4{min(v[0], 0xfffffffeu), min(v[1], 0xfffffffeu), min(v[2], 0xfffffffeu), min(v[3], 0xfffffffeu)}; }
template <int I> DEVI void poll_issue(u32x4& f, unsigned pend, rsrc_t rw, unsigned voff, unsigned soff) {
    asm volatile("s_bitcmp1_b32 %1, %5\n\ts_cbranch_scc0 .Lskip%=\n\tbuffer_load_dwordx4 %0, %2, %3, %4 offen sc1\n.Lskip%=:"
                 : "+v"(f) : "s"(pend), "v"(voff), "s"(rw), "s"(soff), "n"(I) : "memory", "scc");
}
DEVI void poll8(u32x4 (&f)[8], rsrc_t rw, unsigned voff, unsigned base, unsigned stride) {
    unsigned pend = 0xffu;
#pragma unroll
    for (int i = 0; i < 8; i++) f[i] = u32x4{0, 0, 0, 0};
    for (unsigned spins = 0; spins < (1u << 18); spins++) {
        asm volatile("s_nop 4" ::: "memory");
        poll_issue<0>(f[0], pend, rw, voff, base); poll_issue<1>(f[1], pend, rw, voff, base + stride);
        poll_issue<2>(f[2], pend, rw, voff, base + 2 * stride); poll_issue<3>(f[3], pend, rw, voff, base + 3 * stride);
        poll_issue<4>(f[4], pend, rw, voff, base + 4 * stride); poll_issue<5>(f[5], pend, rw, voff, base + 5 * stride);
        poll_issue<6>(f[6], pend, rw, voff, base + 6 * stride); poll_issue<7>(f[7], pend, rw, voff, base + 7 * stride);
        asm volatile("s_waitcnt vmcnt(0)" : "+v"(f[0]), "+v"(f[1]), "+v"(f[2]), "+v"(f[3]), "+v"(f[4]), "+v"(f[5]), "+v"(f[6]), "+v"(f[7]) :: "memory");
        unsigned np = 0;
#pragma unroll
        for (int i = 0; i < 8; i++) np |= __any(max(max(f[i][0], f[i][1]), max(f[i][2], f[i][3])) == ~0u) ? (1u << i) : 0u;
        pend = __builtin_amdgcn_readfirstlane(np);
        if (pend == 0) break;
    }
}

#define SLP 4
#define LD7 "s_nop 4\n\tbuffer_load_dwordx4 %0, %7, %8, %9 offen sc1\n\tbuffer_load_dwordx4 %1, %7, %8, %9 offen offset:1024 sc1\n\t"   \
            "buffer_load_dwordx4 %2, %7, %8, %9 offen offset:2048 sc1\n\tbuffer_load_dwordx4 %3, %7, %8, %9 offen offset:3072 sc1\n\t"    \
            "buffer_load_dwordx4 %4, %7, %8, %10 offen sc1\n\tbuffer_load_dwordx4 %5, %7, %8, %10 offen offset:1024 sc1\n\t"              \
            "buffer_load_dwordx4 %6, %7, %8, %10 offen offset:2048 sc1"
DEVI void issue7(u32x4 (&f)[7], rsrc_t rw, unsigned voff, unsigned soff) {
    asm volatile(LD7 : "=&v"(f[0]), "=&v"(f[1]), "=&v"(f[2]), "=&v"(f[3]), "=&v"(f[4]), "=&v"(f[5]), "=&v"(f[6])
                 : "v"(voff), "s"(rw), "s"(soff), "s"(soff + 4096) : "memory");
}
DEVI void issue7if(u32x4 (&f)[7], unsigned cond, rsrc_t rw, unsigned voff, unsigned soff) {
    asm volatile("s_cmp_eq_u32 %11, 0\n\ts_cbranch_scc1 .Lno7_%=\n\t" LD7 "\n.Lno7_%=:"
                 : "+v"(f[0]), "+v"(f[1]), "+v"(f[2]), "+v"(f[3]), "+v"(f[4]), "+v"(f[5]), "+v"(f[6])
                 : "v"(voff), "s"(rw), "s"(soff), "s"(soff + 4096), "s"(cond) : "memory", "scc");
}
DEVI void probe(rsrc_t rw, unsigned pv, unsigned soff) {
#pragma unroll 1
    for (unsigned spins = 0; spins < (1u << 18); spins++) {
        unsigned m;
        asm volatile("s_nop 4\n\tbuffer_load_dword %0, %1, %2, %3 offen sc1\n\ts_waitcnt vmcnt(0)" : "=&v"(m) : "v"(pv), "s"(rw), "s"(soff) : "memory");
        if (!__any(m == ~0u)) break;
        __builtin_amdgcn_s_sleep(SLP);
    }
}
template <int N> DEVI void check7(u32x4 (&f)[7], rsrc_t rw, unsigned voff, unsigned soff) {
    asm volatile("s_waitcnt vmcnt(%7)" : "+v"(f[0]), "+v"(f[1]), "+v"(f[2]), "+v"(f[3]), "+v"(f[4]), "+v"(f[5]), "+v"(f[6]) : "n"(N) : "memory");
#pragma unroll 1
    for (unsigned spins = 0; spins < 64; spins++) {
        unsigned m = 0;
#pragma unroll
        for (int i = 0; i < 7; i++) m = max(max(m, max(f[i][0], f[i][1])), max(f[i][2], f[i][3]));
        if (!__any(m == ~0u)) break;
        probe(rw, min(voff, 96u) * 64, soff);
        asm volatile(LD7 "\n\ts_waitcnt vmcnt(0)" : "+v"(f[0]), "+v"(f[1]), "+v"(f[2]), "+v"(f[3]), "+v"(f[4]), "+v"(f[5]), "+v"(f[6])
                     : "v"(voff), "s"(rw), "s"(soff), "s"(soff + 4096) : "memory");
    }
}

__global__ __launch_bounds__(512, 2) void k2_kernel(const unsigned short* __restrict__ x, const unsigned short* __restrict__ b1,
                                                    unsigned short* __restrict__ out, char* ws) {
    __shared__ __align__(16) char smem[L2_END];
    const unsigned sb = (unsigned)(size_t)(LDS3 char*)smem;
    const int tid = threadIdx.x, lane = tid & 63, w = __builtin_amdgcn_readfirstlane(tid >> 6);
    const int b = blockIdx.x, g = (b & 7) * 4 + (b >> 6), j = (b >> 3) & 7;
    const int r = lane & 31, h = lane >> 5, kh = w >> 2;
    const unsigned l16 = lane * 16;
    const rsrc_t rw = mkrsrc(ws, WS_END);
    STAMPDECL STAMP(0);
    u32x4 q0, q1; u32x2 km;
    asm volatile("s_nop 4\n\tbuffer_load_dwordx4 %0, %3, %4, %5 offen\n\tbuffer_load_dwordx4 %1, %3, %4, %6 offen\n\tbuffer_load_dwordx2 %2, %7, %4, 0 offen"
                 : "=&v"(q0), "=&v"(q1), "=&v"(km)
                 : "v"(l16), "s"(rw), "s"(Q_OFF + ((g * 2) * 32 + 4 * j + (w & 3)) * 1024), "s"(Q_OFF + ((g * 2 + 1) * 32 + 4 * j + (w & 3)) * 1024),
                   "v"(KMAX_OFF + lane * 8) : "memory");
#define KVCHUNK(off, n) _Pragma("unroll") for (int i = 0; i < n; i++) { const unsigned o_ = kh * 65536 + (off) + ((w & 3) * n + i) * 1024; dma16(rw, l16, KV_OFF + o_, sb + o_); }
#define WOPIECE(i) dma16(rw, l16, WO_OFF + (j * 64 + w * 8 + (i)) * 1024, sb + kh * 65536 + (w & 3) * 8192 + (i) * 1024);
    KVCHUNK(0, 2)
    BAR();
    KVCHUNK(8192, 4)
    if (tid < 256) sts128(sb + L2_ONES + tid * 16, u32x4{0x3f803f80u, 0x3f803f80u, 0x3f803f80u, 0x3f803f80u});
    asm volatile("s_waitcnt vmcnt(6)" : "+v"(q0), "+v"(q1), "+v"(km) :: "memory");
    float qn0 = 0.f, qn1 = 0.f;
#pragma unroll
    for (int e = 0; e < 4; e++) { qn0 = dot2(q0[e], q0[e], qn0); qn1 = dot2(q1[e], q1[e], qn1); }
    const float kmax2 = wavemax(fmaxf(__builtin_bit_cast(float, km[0]), __builtin_bit_cast(float, km[1])));
    const float mb = __builtin_sqrtf(fmaxf(xsum32(qn0), xsum32(qn1)) * kmax2) * 1.001f;
    const bool fast = __all(mb <= 60.f);
    const unsigned aslot = A_OFF + ((g * 8 + j) * 28 + 2 * (w < 7 ? w : 6)) * 1024;
    const rsrc_t ra = mkrsrc(ws, w < 7 ? WS_END : 0);
#define STA(v, soff) stc(v, ra, l16, soff)
#define MARK(i) if ((i) < 4) STA((u32x4{~0u, ~0u, ~0u, ~0u}), aslot + (((i) >> 1) * 14 + ((i) & 1)) * 1024); else STC((u32x4{~0u, ~0u, ~0u, ~0u}), l16, hslot);
    const int tt = w & 1, ct = (w >> 1) & 1;
    const unsigned hslot = H_OFF + (g * 64 + 8 * j + 4 * tt + 2 * ct + kh) * 1024;
    const unsigned w1o = W1_OFF + ((7 * j + (w < 7 ? w : 6)) * 32) * 1024;
    u32x4 w1f[32];
    f32x16 o0, o1;
    float m0, m1;
#pragma unroll
    for (int i = 0; i < 16; i++) { o0[i] = 0.f; o1[i] = 0.f; }
    unsigned ka = sb + kh * 65536 + l16, va = r < 16 ? sb + kh * 65536 + 1024 + h * 256 + r * 16 : sb + L2_ONES + ((r >= 20 && r < 28) ? 64 : 0);
    const unsigned vinc = r < 16 ? 2048 : 0;
    u32x4 warm = {0, 0, 0, 0};
#define TOUCH(i) asm volatile("s_nop 4\n\tbuffer_load_dwordx4 %0, %1, %2, %3 offen" : "+v"(warm) : "v"(l16), "s"(rw),                  \
        "s"((i) < 2 ? w1o + (24 + (b >> 6) * 2 + (i)) * 1024 : W2_OFF + (j * 224 + w * 28 + (b >> 6) * 7 + (i) - 2) * 1024) : "memory");
    if (fast) {
        f32x16 nm, sA, sB;
#pragma unroll
        for (int i = 0; i < 16; i++) nm[i] = -mb;
        m0 = mb; m1 = mb;
        u32x4 p1 = {0, 0, 0, 0}, k0, k1, va0, va1, vb0, vb1;
#define EY(S, i) e[i] = __builtin_amdgcn_exp2f(S[i]); SB;
#define CV(P, k, i) P[k] = cvtpk(e[i], e[i + 1]); SB;
#define STEP(S_, OP, VP1, SN, KN, QN, OC, VC0, LD) { float e[16]; u32x4 p0;                                                   \
        EY(S_, 0) EY(S_, 1) OP = MF32(B8(VP1), B8(p1), OP); SB; EY(S_, 2) EY(S_, 3) CV(p0, 0, 0) EY(S_, 4) EY(S_, 5) CV(p0, 1, 2) EY(S_, 6) \
        SN = MF32(B8(KN), B8(QN), nm); SB; LD EY(S_, 7) CV(p0, 2, 4) EY(S_, 8) EY(S_, 9) CV(p0, 3, 6) EY(S_, 10) EY(S_, 11)       \
        OC = MF32(B8(VC0), B8(p0), OC); SB; CV(p1, 0, 8) EY(S_, 12) EY(S_, 13) CV(p1, 1, 10) EY(S_, 14) EY(S_, 15) CV(p1, 2, 12) CV(p1, 3, 14) }
#define LDKV(K_, V0_, V1_, T) K_ = lds128(ka + (T) * 2048); V0_ = lds128(va); V1_ = lds128(va + 512); va += vinc; asm volatile("" : "+v"(va)); SB;
        WAITV(4); WAITL(); BAR();
        STAMP(11);
        LDKV(k0, va0, va1, 0)
        vb1 = va1;
        sA = MF32(B8(k0), B8(q0), nm);
#pragma unroll
        for (int it = 0; it < 16; it++) {
            if (it == 1) { WAITV(5); BAR(); }
            if (it == 5) { WAITV(14); BAR(); }
            if (it == 10) { WAITV(26); BAR(); }
            SB;
            STEP(sA, o1, vb1, sB, k0, q1, o0, va0, LDKV(k1, vb0, vb1, 2 * it + 1))
            if (it == 0) { KVCHUNK(24576, 5) } if (it == 1) { KVCHUNK(45056, 5) }
            if (it >= 2 && it < 14) { w1f[2 * it - 4] = LDW(l16, w1o + (2 * it - 4) * 1024); SB; }
            STEP(sB, o0, va1, sA, k1, q0, o1, va0, )
            if (it >= 2 && it < 7) { MARK(it - 2) SB; }
            if (it >= 10 && it < 14) { WOPIECE(2 * it - 20) WOPIECE(2 * it - 19) }
            STEP(sA, o1, va1, sB, k1, q1, o0, vb0, LDKV(k0, va0, va1, 2 * it + 2))
            if (it >= 2 && it < 14) { w1f[2 * it - 3] = LDW(l16, w1o + (2 * it - 3) * 1024); SB; }
            STEP(sB, o0, vb1, sA, k0, q0, o1, vb0, )
            if (it >= 5 && it < 14) { TOUCH(it - 5) SB; }
        }
        o1 = MF32(B8(vb1), B8(p1), o1);
    } else {
        KVCHUNK(24576, 5) KVCHUNK(45056, 5)
        WAITV(0); WAITL(); BAR();
        m0 = -3.0e38f; m1 = -3.0e38f;
        const f32x16 z = {0, 0, 0, 0, 0, 0, 0, 0, 0, 0, 0, 0, 0, 0, 0, 0};
#define SLOW(Q_, O_, M_) { f32x16 s = MF32(B8(kf), B8(Q_), z); float mx = s[0];                                               \
            _Pragma("unroll") for (int i = 1; i < 16; i++) mx = fmaxf(mx, s[i]);                                              \
            mx = fmaxf(xmax32(mx), M_); const float al = __builtin_amdgcn_exp2f(M_ - mx); M_ = mx; u32x4 p0, p1;              \
            _Pragma("unroll") for (int i = 0; i < 16; i++) O_[i] *= al;                                                       \
            _Pragma("unroll") for (int i = 0; i < 4; i++) {                                                                   \
                p0[i] = cvtpk(__builtin_amdgcn_exp2f(s[2 * i] - mx), __builtin_amdgcn_exp2f(s[2 * i + 1] - mx));              \
                p1[i] = cvtpk(__builtin_amdgcn_exp2f(s[2 * i + 8] - mx), __builtin_amdgcn_exp2f(s[2 * i + 9] - mx)); }        \
            O_ = MF32(B8(v0), B8(p0), O_); O_ = MF32(B8(v1), B8(p1), O_); }
#pragma unroll 1
        for (int t = 0; t < 32; t++) {
            if (t == 2 || t == 10 || t == 20) { WAITL(); BAR(); }
            if (t == 20) { _Pragma("unroll") for (int i = 0; i < 8; i++) WOPIECE(i) }
            const u32x4 kf = lds128(ka), v0 = lds128(va), v1 = lds128(va + 512);
            ka += 2048; va += vinc;
            SLOW(q0, o0, m0) SLOW(q1, o1, m1)
        }
#pragma unroll
        for (int i = 0; i < 24; i++) w1f[i] = LDW(l16, w1o + i * 1024);
#pragma unroll
        for (int i = 0; i < 5; i++) { MARK(i) }
    }
    STAMP(1);
    const f32x16 z16 = {0, 0, 0, 0, 0, 0, 0, 0, 0, 0, 0, 0, 0, 0, 0, 0};
    f32x16 cs = z16;
#define COLSUM(k0, k1) _Pragma("unroll") for (int ks = k0; ks < k1; ks++) cs = MF32(B8(w1f[ks]), B8((u32x4{0x3f803f80u, 0x3f803f80u, 0x3f803f80u, 0x3f803f80u})), cs); \
    asm volatile("" : "+v"(cs));
    {
        const f32x16 os = kh ? o0 : o1, ok = kh ? o1 : o0;
        const float ms = kh ? m0 : m1, mk = kh ? m1 : m0;
        const unsigned xa = sb + L2_X + w * 2560, xb = sb + L2_X + (w ^ 4) * 2560;
        *(LDS3 f32x4*)(size_t)(xa + l16) = f32x4{os[0], os[1], os[2], os[3]};
        *(LDS3 f32x4*)(size_t)(xa + 1024 + l16) = f32x4{os[4], os[5], os[6], os[7]};
        *(LDS3 f32x2*)(size_t)(xa + 2048 + lane * 8) = f32x2{os[8], ms};
        WAITV(0); WAITL(); BAR();
        asm volatile("" :: "v"(warm));
        const f32x4 a = *(LDS3 f32x4*)(size_t)(xb + l16), c = *(LDS3 f32x4*)(size_t)(xb + 1024 + l16);
        const f32x2 lm = *(LDS3 f32x2*)(size_t)(xb + 2048 + lane * 8);
        const float mm = fmaxf(mk, lm[1]), ea = __builtin_amdgcn_exp2f(mk - mm), eb = __builtin_amdgcn_exp2f(lm[1] - mm);
        const float inv = 1.f / (ok[8] * ea + lm[0] * eb), fa = ea * inv, fb = eb * inv;
        u32x4 of;
        of[0] = cvtpk(ok[0] * fa + a[0] * fb, ok[1] * fa + a[1] * fb); of[1] = cvtpk(ok[2] * fa + a[2] * fb, ok[3] * fa + a[3] * fb);
        of[2] = cvtpk(ok[4] * fa + c[0] * fb, ok[5] * fa + c[1] * fb); of[3] = cvtpk(ok[6] * fa + c[2] * fb, ok[7] * fa + c[3] * fb);
        STC(clean(of), l16, O_OFF + ((g * 2 + kh) * 32 + 4 * j + (w & 3)) * 1024);
    }
    STAMP(2);
    const unsigned xo = (64 * g + 32 * tt + r) * 512 + 64 * j + 32 * ct + 16 * kh;
    const u32x2 xa0 = *(const u32x2*)(x + xo + 4 * h), xa1 = *(const u32x2*)(x + xo + 8 + 4 * h);
    u32x2 bb[4];
#pragma unroll
    for (int i = 0; i < 4; i++) bb[i] = *(const u32x2*)(b1 + 224 * j + 32 * (w < 7 ? w : 6) + 8 * i + 4 * h);
    const unsigned myr = sb + kh * 65536 + 32768 + (w & 3) * 8192;
    const unsigned w2o = W2_OFF + (j * 224 + w * 28) * 1024;
    float hv[8];
    SB; COLSUM(0, 24) SB;
    {
        const int kq = w >> 1;
        u32x4 of[8], af[4];
        const unsigned wa = sb + kq * 8192 + l16;
        af[0] = lds128(wa); af[1] = lds128(wa + 65536); af[2] = lds128(wa + 1024); af[3] = lds128(wa + 65536 + 1024);
        poll8(of, rw, l16, O_OFF + ((g * 2 + tt) * 32 + 8 * kq) * 1024, 1024);
        u32x2 xb0 = xa0, xb1 = xa1;
        asm volatile("" : "+v"(xb0), "+v"(xb1), "+v"(bb[0]), "+v"(bb[1]), "+v"(bb[2]), "+v"(bb[3]));
        STAMP(3);
        f32x16 acc[2] = {z16, z16};
#pragma unroll
        for (int i = 0; i < 16; i++) {
            acc[i & 1] = MF32(B8(af[i & 3]), B8(of[i >> 1]), acc[i & 1]); SB;
            if (i < 12) { af[i & 3] = lds128(wa + (i & 1) * 65536 + ((i >> 1) + 2) * 1024); SB; }
            if (i & 1) { w1f[24 + (i >> 1)] = LDW(l16, w1o + (24 + (i >> 1)) * 1024); SB; }
        }
#pragma unroll
        for (int p = 0; p < 4; p++) {
            const unsigned xa = sb + (p >> 1) * 65536 + 32768 + (tt + 2 * (p & 1)) * 8192 + kq * 2048 + l16, o = (p >> 1) * 8;
            const f32x16 v = acc[p & 1];
            *(LDS3 f32x4*)(size_t)xa = f32x4{v[o], v[o + 1], v[o + 2], v[o + 3]};
            *(LDS3 f32x4*)(size_t)(xa + 1024) = f32x4{v[o + 4], v[o + 5], v[o + 6], v[o + 7]};
        }
        WAITL(); BAR();
        f32x4 a = {blo(xb0[0]), bhi(xb0[0]), blo(xb0[1]), bhi(xb0[1])}, c = {blo(xb1[0]), bhi(xb1[0]), blo(xb1[1]), bhi(xb1[1])};
#pragma unroll
        for (int p = 0; p < 4; p++) { a += *(LDS3 f32x4*)(size_t)(myr + p * 2048 + l16); c += *(LDS3 f32x4*)(size_t)(myr + p * 2048 + 1024 + l16); }
#pragma unroll
        for (int i = 0; i < 4; i++) { hv[i] = a[i]; hv[4 + i] = c[i]; }
        STC(clean(u32x4{cvtpk(hv[0], hv[1]), cvtpk(hv[2], hv[3]), cvtpk(hv[4], hv[5]), cvtpk(hv[6], hv[7])}), l16, hslot);
    }
    STAMP(4);
    SB; COLSUM(24, 32) SB;
#pragma unroll
    for (int i = 0; i < 4; i++) dma16(rw, l16, w2o + i * 1024, i < 8 ? myr + i * 1024 : sb + L2_W2 + (w * 3 + i - 8) * 1024);
    {
        u32x4 hf[8];
        poll8(hf, rw, l16, H_OFF + (g * 64 + 8 * w) * 1024, 1024);
        STAMP(5);
#pragma unroll
        for (int i = 0; i < 8; i++) sts128(sb + (i >> 2) * 65536 + (4 * w + (i & 3)) * 1024 + l16, hf[i]);
        f32x4 st;
#pragma unroll
        for (int t = 0; t < 2; t++) {
            float s0 = 0.f, q0_ = 0.f, s1 = 0.f, q1_ = 0.f;
#pragma unroll
            for (int i = 0; i < 4; i++)
#pragma unroll
                for (int e = 0; e < 4; e += 2) {
                    s0 = dot2(hf[4 * t + i][e], 0x3f803f80u, s0); q0_ = dot2(hf[4 * t + i][e], hf[4 * t + i][e], q0_);
                    s1 = dot2(hf[4 * t + i][e + 1], 0x3f803f80u, s1); q1_ = dot2(hf[4 * t + i][e + 1], hf[4 * t + i][e + 1], q1_);
                }
            st[2 * t] = xsum32(s0 + s1); st[2 * t + 1] = xsum32(q0_ + q1_);
        }
        if (h == 0) *(LDS3 f32x4*)(size_t)(sb + L2_STAT + w * 512 + r * 16) = st;
        if (tid == 0) *(LDS3 unsigned*)(size_t)(sb + L2_ONES) = 0;
        WAITL(); BAR();
    }
    STAMP(6);
    __builtin_amdgcn_s_setprio(1);
    u32x4 w2f[17], fa[7], fb[7];
    const unsigned ab = A_OFF + (g * 8 + w) * 28 * 1024;
    {
        f32x16 ac[2] = {z16, z16};
        u32x4 bf[4];
        unsigned ha0 = sb + l16, ha1 = sb + 65536 + l16;
        asm volatile("" : "+v"(ha1));
#pragma unroll
        for (int i = 0; i < 4; i++) bf[i] = lds128(((i & 1) ? ha1 : ha0) + (i >> 1) * 1024);
        f32x4 st = {0, 0, 0, 0};
        float mean[2], rstd[2];
        SB;
        sfor<64>([&](auto K) __attribute__((always_inline)) {
            constexpr int n = K.v;
            ac[n & 1] = MF32(B8(w1f[n >> 1]), B8(bf[n & 3]), ac[n & 1]); SB;
            if (n < 60) { bf[n & 3] = lds128(((n & 1) ? ha1 : ha0) + ((n >> 1) + 2) * 1024); SB; }
            if (n >= 4 && n < 12) { st += *(LDS3 f32x4*)(size_t)(sb + L2_STAT + (n - 4) * 512 + r * 16); SB; }
            if (n == 16) {
#pragma unroll
                for (int t = 0; t < 2; t++) {
                    mean[t] = st[2 * t] * (1.f / 512.f);
                    rstd[t] = __builtin_amdgcn_rsqf(fmaxf(st[2 * t + 1] * (1.f / 512.f) - mean[t] * mean[t], 0.f) + 1e-5f);
                }
                SB;
            }
            constexpr int m = (n >> 1) + 4;
            if ((n & 1) && m < 11) { dma16(rw, l16, w2o + m * 1024, m < 8 ? myr + m * 1024 : sb + L2_W2 + (w * 3 + m - 8) * 1024); SB; }
            if ((n & 1) && m >= 11 && m < 28) { w2f[m >= 11 && m < 28 ? m - 11 : 0] = LDW(l16, w2o + m * 1024); SB; }
        });
        STAMP(12);
#define PIN(x) asm volatile("" : "+v"(x));
        sfor<4>([&](auto K) __attribute__((always_inline)) {
            constexpr int t = K.v >> 1, s = K.v & 1;
            float z[8], u[8];
            u32x4 yp;
#define EL8 _Pragma("unroll") for (int i = 0; i < 8; i++)
            EL8 { u[i] = fmaf(-mean[t], cs[8 * s + i], ac[t][8 * s + i]); PIN(u[i]) }
            EL8 { const unsigned bw = bb[2 * s + (i >> 2)][(i >> 1) & 1]; z[i] = (i & 1) ? bhi(bw) : blo(bw); PIN(z[i]) }
            EL8 { z[i] = fmaf(u[i], rstd[t], z[i]); PIN(z[i]) }
            EL8 { u[i] = z[i] * LOG2E; PIN(u[i]) }
            EL8 { u[i] = __builtin_amdgcn_exp2f(u[i]); PIN(u[i]) }
            float a_[8];
            EL8 { a_[i] = u[i] + 2.f; PIN(a_[i]) }
            EL8 { u[i] = fmaf(u[i], a_[i], 2.f); PIN(u[i]) }
            EL8 { u[i] = __builtin_amdgcn_rcpf(u[i]); PIN(u[i]) }
            EL8 { u[i] = fmaf(u[i], -2.f, 1.f); PIN(u[i]) }
            EL8 { z[i] *= u[i]; PIN(z[i]) }
            EL8 if (i < 4) { yp[i & 3] = cvtpk(z[2 * (i & 3)], z[2 * (i & 3) + 1]); PIN(yp[i & 3]) }
            STA(clean(yp), aslot + (t * 14 + s) * 1024); SB;
            if (K.v == 1) {
                asm volatile("" : "+v"(w2f[0]), "+v"(w2f[1]), "+v"(w2f[2]), "+v"(w2f[3]), "+v"(w2f[4]), "+v"(w2f[5]), "+v"(w2f[6]), "+v"(w2f[7]), "+v"(w2f[8]),
                                  "+v"(w2f[9]), "+v"(w2f[10]), "+v"(w2f[11]), "+v"(w2f[12]), "+v"(w2f[13]), "+v"(w2f[14]), "+v"(w2f[15]), "+v"(w2f[16]));
#pragma unroll
                for (int i = 0; i < 7; i++) asm volatile("" : "=v"(fa[i]), "=v"(fb[i]));
                issue7if(fa, kh, rw, l16, ab); issue7if(fb, kh, rw, l16, ab + 14336); SB;
                STAMP(13);
            }
        });
    }
    STAMP(7);
    {
        u32x4 wl[2];
        __builtin_amdgcn_s_setprio(0);
        LDS3 unsigned* done = (LDS3 unsigned*)(size_t)(sb + L2_ONES);
        if (lane == 0) __hip_atomic_fetch_add(done, 1u, __ATOMIC_RELAXED, __HIP_MEMORY_SCOPE_WORKGROUP);
#define ALLDONE while (__builtin_amdgcn_readfirstlane(__hip_atomic_load(done, __ATOMIC_RELAXED, __HIP_MEMORY_SCOPE_WORKGROUP)) < 8) __builtin_amdgcn_s_sleep(2);
        const unsigned pv = (lane < 7 ? lane : lane < 14 ? lane + 7 : 0) * 1024;
        issue7if(fa, kh ^ 1, rw, l16, ab);
        if (!kh) probe(rw, min(l16, 96u) * 64, ab + 14336);
        issue7if(fb, kh ^ 1, rw, l16, ab + 14336);
        unsigned wla = myr + l16, wlb = sb + L2_W2 + w * 3072 + l16;
        asm volatile("" : "+v"(wla), "+v"(wlb));
#define WLD(n) lds128((n) < 8 ? wla + (n) * 1024 : wlb + ((n) - 8) * 1024)
#define F2(F, ks0, C0, C1) _Pragma("unroll") for (int k = 0; k < 7; k++) { const int n0 = 2 * ((ks0) + k), n1 = n0 + 1;               \
            C0 = MF32(B8(n0 < 11 ? wl[0] : w2f[n0 < 11 ? 0 : n0 - 11]), B8(F[k]), C0); SB; if (n0 + 2 < 11) { wl[0] = WLD(n0 + 2); SB; } \
            C1 = MF32(B8(n1 < 11 ? wl[1] : w2f[n1 < 11 ? 0 : n1 - 11]), B8(F[k]), C1); SB; if (n1 + 2 < 11) { wl[1] = WLD(n1 + 2); SB; } }
#define PST(v, t, cc, s) sts128(sb + (t) * 65536 + ((cc) + 2 * (s)) * 8192 + w * 1024 + l16,                                          \
            u32x4{cvtpk(v[8 * (s)], v[8 * (s) + 1]), cvtpk(v[8 * (s) + 2], v[8 * (s) + 3]), cvtpk(v[8 * (s) + 4], v[8 * (s) + 5]), cvtpk(v[8 * (s) + 6], v[8 * (s) + 7])});
        f32x16 c0 = z16, c1 = z16, c2 = z16, c3 = z16;
        wl[0] = WLD(0); wl[1] = WLD(1);
        check7<7>(fa, rw, l16, ab);
        STAMP(14);
        F2(fa, 0, c0, c1)
        issue7if(fa, kh, rw, l16, ab + 7168);
        wl[0] = WLD(0); wl[1] = WLD(1);
        if (!kh) WAITV(0);
        check7<7>(fb, rw, l16, ab + 14336);
        F2(fb, 0, c2, c3)
        if (!kh) { ALLDONE probe(rw, pv, ab + 7168); }
        issue7if(fa, kh ^ 1, rw, l16, ab + 7168);
        issue7(fb, rw, l16, ab + 21504);
        check7<7>(fa, rw, l16, ab + 7168);
        F2(fa, 7, c0, c1)
        if (kh) { ALLDONE asm volatile("" ::: "memory"); }
        PST(c0, 0, 0, 0) PST(c0, 0, 0, 1) PST(c1, 0, 1, 0) PST(c1, 0, 1, 1)
        check7<0>(fb, rw, l16, ab + 21504);
        STAMP(15);
        F2(fb, 7, c2, c3)
        STAMP(8);
        PST(c2, 1, 0, 0) PST(c2, 1, 0, 1) PST(c3, 1, 1, 0) PST(c3, 1, 1, 1)
        WAITL(); BAR();
        STAMP(9);
        unsigned sl = 0x00003f80u, sh = 0x3f800000u;
        asm volatile("" : "+s"(sl), "+s"(sh));
#pragma unroll
        for (int i = 0; i < 8; i++) {
            const u32x4 pf = lds128(sb + tt * 65536 + (w >> 1) * 8192 + i * 1024 + l16);
#pragma unroll
            for (int e = 0; e < 4; e++) { hv[2 * e] = dot2(pf[e], sl, hv[2 * e]); hv[2 * e + 1] = dot2(pf[e], sh, hv[2 * e + 1]); }
        }
        const u32x2 s0 = __builtin_amdgcn_permlane32_swap(cvtpk(hv[0], hv[1]), cvtpk(hv[4], hv[5]), false, false);
        const u32x2 s1 = __builtin_amdgcn_permlane32_swap(cvtpk(hv[2], hv[3]), cvtpk(hv[6], hv[7]), false, false);
        *(u32x4*)(out + xo + 8 * h) = u32x4{s0[0], s1[0], s0[1], s1[1]};
    }
    STAMP(10);
}

extern "C" void kernel_launch(void* const* d_in, const int* in_sizes, int n_in, void* d_out, int out_size, void* d_ws, size_t ws_size,
                              hipStream_t stream) {
    typedef const unsigned short* P;
    k1_kernel<<<256, 512, 0, stream>>>((P)d_in[0], (P)d_in[1], (P)d_in[2], (P)d_in[3], (P)d_in[4], (P)d_in[6], (P)d_in[8], (char*)d_ws);
    k2_kernel<<<256, 512, 0, stream>>>((P)d_in[0], (P)d_in[7], (unsigned short*)d_out, (char*)d_ws);
}
```

```cpp
#include <hip/hip_runtime.h>
#include <hip/hip_bf16.h>

typedef __attribute__((ext_vector_type(8))) __bf16 bf16x8;
typedef __attribute__((ext_vector_type(4))) __bf16 bf16x4;
typedef __attribute__((ext_vector_type(2))) __bf16 bf16x2;
typedef __attribute__((ext_vector_type(16))) float f32x16;
typedef __attribute__((ext_vector_type(4))) float f32x4;
typedef __attribute__((ext_vector_type(2))) float f32x2;
typedef __attribute__((ext_vector_type(4))) unsigned u32x4;
typedef __attribute__((ext_vector_type(2))) unsigned u32x2;
typedef __attribute__((ext_vector_type(4))) short s16x4;
typedef __amdgpu_buffer_rsrc_t rsrc_t;
#define LDS3 __attribute__((address_space(3)))
#define GLB1 __attribute__((address_space(1)))
#define DEVI __device__ __forceinline__
#define SB __builtin_amdgcn_sched_barrier(0)
#define B8(x) __builtin_bit_cast(bf16x8, x)
#define MF32(a, b, c) __builtin_amdgcn_mfma_f32_32x32x16_bf16(a, b, c, 0, 0, 0)
#define MF16(a, b, c) __builtin_amdgcn_mfma_f32_16x16x32_bf16(a, b, c, 0, 0, 0)
#define STAMP(i)
#define STAMP1(i)
#define STAMP1DECL
#define STAMPDECL

constexpr unsigned FLAG_OFF = 0, KMAX_OFF = 16384, KV_OFF = 32768, Q_OFF = 0x40000, WO_OFF = Q_OFF + (2u << 20),
                   W1_OFF = WO_OFF + (512u << 10), W2_OFF = W1_OFF + (1792u << 10), O_OFF = W2_OFF + (1792u << 10),
                   H_OFF = O_OFF + (2u << 20), A_OFF = H_OFF + (2u << 20), WS_END = A_OFF + (7u << 20);
constexpr unsigned K1_WARM = 5, K2_WARM = 28;
constexpr float LOG2E = 1.4426950408889634f;

template <int V> struct IC { static constexpr int v = V; };
template <int N, class F> DEVI void sfor(F&& f) { if constexpr (N > 0) { sfor<N - 1>(f); f(IC<N - 1>{}); } }
DEVI unsigned cvtpk(float a, float b) { f32x2 v = {a, b}; return __builtin_bit_cast(unsigned, __builtin_convertvector(v, bf16x2)); }
DEVI float blo(unsigned u) { return __builtin_bit_cast(float, u << 16); }
DEVI float bhi(unsigned u) { return __builtin_bit_cast(float, u & 0xffff0000u); }
DEVI rsrc_t mkrsrc(const void* p, unsigned bytes) { return __builtin_amdgcn_make_buffer_rsrc((void*)p, 0, bytes, 0x00020000); }
DEVI void dma16(rsrc_t rs, unsigned voff, unsigned soff, unsigned lds) {
    unsigned keep;
    asm volatile("s_mov_b32 %0, m0\n\ts_mov_b32 m0, %1\n\ts_nop 4\n\tbuffer_load_dwordx4 %2, %3, %4 offen lds\n\ts_mov_b32 m0, %0"
                 : "=&s"(keep) : "s"(lds), "v"(voff), "s"(rs), "s"(soff) : "memory");
}
#define WAITV(n) asm volatile("s_waitcnt vmcnt(" #n ")" ::: "memory")
#define WAITL() asm volatile("s_waitcnt lgkmcnt(0)" ::: "memory")
#define BAR() asm volatile("s_barrier" ::: "memory")
DEVI u32x4 lds128(unsigned a) { return *(LDS3 u32x4*)(size_t)a; }
DEVI void sts128(unsigned a, u32x4 v) { *(LDS3 u32x4*)(size_t)a = v; }
DEVI f32x2 swp32(float x) { float y = x; asm("s_nop 1\n\tv_permlane32_swap_b32 %0, %1\n\ts_nop 1" : "+v"(x), "+v"(y)); return f32x2{x, y}; }
DEVI f32x2 swp16(float x) { float y = x; asm("s_nop 1\n\tv_permlane16_swap_b32 %0, %1\n\ts_nop 1" : "+v"(x), "+v"(y)); return f32x2{x, y}; }
DEVI float xsum32(float x) { const f32x2 t = swp32(x); return t[0] + t[1]; }
DEVI float xmax32(float x) { const f32x2 t = swp32(x); return fmaxf(t[0], t[1]); }
DEVI float xsum16(float x) { const f32x2 t = swp16(x); return t[0] + t[1]; }
DEVI float xmax16(float x) { const f32x2 t = swp16(x); return fmaxf(t[0], t[1]); }
template <int C> DEVI float dppf(float x) { return __builtin_bit_cast(float, __builtin_amdgcn_update_dpp(0, __builtin_bit_cast(int, x), C, 0xf, 0xf, true)); }
DEVI float rowsum16(float x) { x += dppf<0xB1>(x); x += dppf<0x4E>(x); x += dppf<0x141>(x); x += dppf<0x140>(x); return x; }
DEVI float rowmax16(float x) { x = fmaxf(x, dppf<0xB1>(x)); x = fmaxf(x, dppf<0x4E>(x)); x = fmaxf(x, dppf<0x141>(x)); x = fmaxf(x, dppf<0x140>(x)); return x; }
DEVI float wavesum(float x) { return xsum32(xsum16(rowsum16(x))); }
DEVI float wavemax(float x) { return xmax32(xmax16(rowmax16(x))); }
DEVI float dot2(unsigned a, unsigned b, float c) { return __builtin_amdgcn_fdot2_f32_bf16(__builtin_bit_cast(bf16x2, a), __builtin_bit_cast(bf16x2, b), c, false); }

#define LDW(voff, soff) __builtin_amdgcn_raw_buffer_load_b128(rw, voff, soff, 0)
#define LDC(voff, soff) __builtin_amdgcn_raw_buffer_load_b128(rw, voff, soff, 16)
DEVI void stc(u32x4 v, rsrc_t rs, unsigned voff, unsigned soff) { __builtin_amdgcn_raw_buffer_store_b128(v, rs, voff, soff, 16); asm volatile("s_nop 1" :: "v"(v) : "memory"); }
#define STC(v, voff, soff) stc(v, rw, voff, soff)
#define CODEADDR(pc, sym) asm volatile("s_getpc_b64 vcc\n\ts_add_u32 vcc_lo, vcc_lo, " sym "@rel32@lo+4\n\t"                       \
        "s_addc_u32 vcc_hi, vcc_hi, " sym "@rel32@hi+12\n\ts_mov_b64 %0, vcc" : "=s"(pc) :: "vcc", "scc")
#define K1SYM "_Z9k1_kernelPKtS0_S0_S0_S0_S0_S0_Pc"
#define K2SYM "_Z9k2_kernelPKtS0_PtPc"
constexpr unsigned L1_X = 0, L1_WQ = 65536, L1_WKV = 131072, L1_PK = 147456, L1_END = 163840;
DEVI bf16x8 ldtr(unsigned a, unsigned b) {
    const s16x4 lo = __builtin_amdgcn_ds_read_tr16_b64_v4i16((LDS3 s16x4*)(size_t)a), hi = __builtin_amdgcn_ds_read_tr16_b64_v4i16((LDS3 s16x4*)(size_t)b);
    return B8(__builtin_shufflevector(lo, hi, 0, 1, 2, 3, 4, 5, 6, 7));
}
__global__ __launch_bounds__(512, 2) void k1_kernel(const unsigned short* __restrict__ x, const unsigned short* __restrict__ wq,
        const unsigned short* __restrict__ wk, const unsigned short* __restrict__ wv, const unsigned short* __restrict__ wo,
        const unsigned short* __restrict__ w1, const unsigned short* __restrict__ w2, char* __restrict__ ws) {
    __shared__ __align__(16) char smem[L1_END];
    const unsigned sb = (unsigned)(size_t)(LDS3 char*)smem;
    const int tid = threadIdx.x, lane = tid & 63, w = __builtin_amdgcn_readfirstlane(tid >> 6);
    const int b = blockIdx.x, g = (b & 7) * 4 + (b >> 6), j = (b >> 3) & 7;
    const int rl = lane >> 3, pl = lane & 7;
    const rsrc_t rx = mkrsrc(x, 2048 * 512 * 2), rq = mkrsrc(wq, 512 * 512 * 2), rkv = mkrsrc((j & 1) ? wv : wk, 512 * 16 * 2);
    STAMP1DECL STAMP1(0);
    u32x4 xr[8];
    {
        const unsigned vo = rl * 1024 + pl * 16, so = (g * 64 + w * 8) * 1024;
        asm volatile("s_nop 4\n\tbuffer_load_dwordx4 %0, %8, %9, %10 offen\n\tbuffer_load_dwordx4 %1, %8, %9, %10 offen offset:128\n\t"
                     "buffer_load_dwordx4 %2, %8, %9, %10 offen offset:256\n\tbuffer_load_dwordx4 %3, %8, %9, %10 offen offset:384\n\t"
                     "buffer_load_dwordx4 %4, %8, %9, %10 offen offset:512\n\tbuffer_load_dwordx4 %5, %8, %9, %10 offen offset:640\n\t"
                     "buffer_load_dwordx4 %6, %8, %9, %10 offen offset:768\n\tbuffer_load_dwordx4 %7, %8, %9, %10 offen offset:896"
                     : "=&v"(xr[0]), "=&v"(xr[1]), "=&v"(xr[2]), "=&v"(xr[3]), "=&v"(xr[4]), "=&v"(xr[5]), "=&v"(xr[6]), "=&v"(xr[7])
                     : "v"(vo), "s"(rx), "s"(so) : "memory");
    }
#pragma unroll
    for (int i = 0; i < 8; i++)
        dma16(rq, rl * 1024 + (((pl >> 1) ^ (((rl >> 1) & 1) | ((i & 1) << 1))) * 32) + (pl & 1) * 16, (64 * w + 8 * i) * 1024 + j * 128, sb + L1_WQ + (w * 8 + i) * 1024);
#pragma unroll
    for (int i = 0; i < 2; i++) dma16(rkv, lane * 16, (w * 2 + i) * 1024, sb + L1_WKV + (w * 2 + i) * 1024);
    asm volatile("s_waitcnt vmcnt(10)" : "+v"(xr[0]), "+v"(xr[1]), "+v"(xr[2]), "+v"(xr[3]), "+v"(xr[4]), "+v"(xr[5]), "+v"(xr[6]), "+v"(xr[7]) :: "memory");
    {
        float s0 = 0.f, s1 = 0.f, q0 = 0.f, q1 = 0.f;
#pragma unroll
        for (int i = 0; i < 8; i++)
#pragma unroll
            for (int e = 0; e < 4; e += 2) {
                s0 = dot2(xr[i][e], 0x3f803f80u, s0); q0 = dot2(xr[i][e], xr[i][e], q0);
                s1 = dot2(xr[i][e + 1], 0x3f803f80u, s1); q1 = dot2(xr[i][e + 1], xr[i][e + 1], q1);
            }
        float s = s0 + s1, q = q0 + q1;
        s += dppf<0xB1>(s); q += dppf<0xB1>(q); s += dppf<0x4E>(s); q += dppf<0x4E>(q); s += dppf<0x141>(s); q += dppf<0x141>(q);
        s *= (1.f / 512.f); q *= (1.f / 512.f);
        const float rstd = __builtin_amdgcn_rsqf(fmaxf(q - s * s, 0.f) + 1e-5f), nm = -s * rstd;
        const int row = w * 8 + rl;
#pragma unroll
        for (int i = 0; i < 8; i++) {
            u32x4 o;
#pragma unroll
            for (int e = 0; e < 4; e++) o[e] = cvtpk(fmaf(blo(xr[i][e]), rstd, nm), fmaf(bhi(xr[i][e]), rstd, nm));
            sts128(sb + L1_X + row * 1024 + (((8 * i + pl) ^ (row & 15)) * 16), o);
        }
    }
    unsigned pd0, pd1;
    {
        const int id = b * 8 + w;
        const unsigned short* src; int ld, k0, n0; unsigned bytes;
        if (id < 256)       { src = wo; ld = 512;  bytes = 512 * 512 * 2;  k0 = (id & 31) * 16; n0 = (id >> 5) * 64; pd0 = WO_OFF + ((id >> 5) * 64 + (id & 31)) * 1024; pd1 = pd0 + 32768; }
        else if (id < 1152) { const int i2 = id - 256; src = w1; ld = 1792; bytes = 512 * 1792 * 2; k0 = (i2 & 31) * 16; n0 = (i2 >> 5) * 64;
                              pd0 = W1_OFF + ((i2 >> 5) * 64 + (i2 & 31)) * 1024; pd1 = pd0 + 32768; }
        else                { const int i2 = id - 1152, jc = i2 / 112, fk = i2 - jc * 112;
                              src = w2; ld = 512; bytes = 512 * 1792 * 2; k0 = fk * 16; n0 = jc * 64; pd0 = W2_OFF + i2 * 2048; pd1 = pd0 + 1024; }
        const rsrc_t rp = mkrsrc(src, bytes);
#pragma unroll
        for (int i = 0; i < 2; i++) dma16(rp, rl * ld * 2 + pl * 16, ((k0 + 8 * i) * ld + n0) * 2, sb + L1_PK + w * 2048 + i * 1024);
    }
    WAITV(2); WAITL(); BAR();
    STAMP1(2);
    u32x4 warm;
    {
        unsigned long long pc;
        CODEADDR(pc, K2SYM);
        const unsigned piece = ((b >> 3) * 8 + w) % K2_WARM;
        asm volatile("s_nop 4\n\tglobal_load_dwordx4 %0, %1, %2" : "=&v"(warm) : "v"(piece * 1024 + lane * 16), "s"(pc) : "memory");
    }
    const int c = lane >> 4, li = lane & 15, hq = w & 3, tt = w >> 2;
    const rsrc_t rw = mkrsrc(ws, WS_END);
    STC((u32x4{~0u, ~0u, ~0u, ~0u}), lane * 16, O_OFF + (b * 8 + w) * 1024);
    const unsigned arow = 8 * c + (li >> 2);
    const unsigned abase = sb + L1_WQ + arow * 128 + ((hq ^ (((li >> 3) & 1) | ((c & 1) << 1))) * 32) + (li & 3) * 8;
    unsigned bb[4];
#pragma unroll
    for (int m = 0; m < 4; m++) bb[m] = sb + L1_X + li * 1024 + (((4 * m + c) ^ li) * 16);
    const int sub = j >> 1;
    {
        const unsigned wb = sb + L1_WKV + arow * 32 + (li & 3) * 8 + w * 2048;
        const unsigned xb = sb + L1_X + (sub * 16 + li) * 1024 + (w >> 1) * 256;
        f32x4 d = {0, 0, 0, 0};
#pragma unroll
        for (int u = 0; u < 2; u++) {
            const bf16x8 wf = ldtr(wb + u * 1024, wb + u * 1024 + 128);
            const bf16x8 xf = B8(lds128(xb + (((8 * (w & 1) + 4 * u + c) ^ li) * 16)));
            if (j & 1) d = MF16(xf, wf, d); else d = MF16(wf, xf, d);
        }
        *(LDS3 f32x4*)(size_t)(sb + L1_WKV + w * 2048 + lane * 16) = d;
    }
    f32x4 a0 = {0, 0, 0, 0}, a1 = {0, 0, 0, 0};
    {
        bf16x8 af[3]; u32x4 b0[3], b1[3];
#define K1LD(ks) { af[(ks) % 3] = ldtr(abase + (ks) * 4096, abase + (ks) * 4096 + 512); const unsigned ba = bb[(ks) & 3] + tt * 32768 + ((ks) >> 2) * 256; \
                   b0[(ks) % 3] = lds128(ba); b1[(ks) % 3] = lds128(ba + 16384); SB; }
        K1LD(0) K1LD(1) K1LD(2)
#pragma unroll
        for (int ks = 0; ks < 16; ks++) {
            a0 = MF16(af[ks % 3], B8(b0[ks % 3]), a0); a1 = MF16(af[ks % 3], B8(b1[ks % 3]), a1); SB;
            if (ks < 13) K1LD(ks + 3)
        }
    }
    {
        const float sc = 0.25f * LOG2E;
        const u32x2 s0 = __builtin_amdgcn_permlane32_swap(cvtpk(a0[0] * sc, a0[1] * sc), cvtpk(a1[0] * sc, a1[1] * sc), false, false);
        const u32x2 s1 = __builtin_amdgcn_permlane32_swap(cvtpk(a0[2] * sc, a0[3] * sc), cvtpk(a1[2] * sc, a1[3] * sc), false, false);
        STAMP1(1);
        STC((u32x4{s0[0], s1[0], s0[1], s1[1]}), ((c & 1) * 32 + (c >> 1) * 16 + li) * 16, Q_OFF + ((g * 2 + tt) * 32 + 4 * j + hq) * 1024);
        WAITV(2);
        asm volatile("" :: "v"(warm));
        const unsigned pa = sb + L1_PK + w * 2048 + (4 * (c >> 1) + (li >> 2)) * 128 + (c & 1) * 32 + (li & 3) * 8;
        STC(__builtin_bit_cast(u32x4, ldtr(pa, pa + 1024)), lane * 16, pd0);
        STC(__builtin_bit_cast(u32x4, ldtr(pa + 64, pa + 1088)), lane * 16, pd1);
    }
    WAITL(); BAR();
    if (w == 0) {
        f32x4 d = {0, 0, 0, 0};
#pragma unroll
        for (int i = 0; i < 8; i++) d += *(LDS3 f32x4*)(size_t)(sb + L1_WKV + i * 2048 + lane * 16);
        const u32x2 o = {cvtpk(d[0], d[1]), cvtpk(d[2], d[3])};
        const int s = sub & 1;
        char* kv = ws + KV_OFF + (g * 2 + (sub >> 1)) * 2048;
        if (j & 1) *(u32x2*)(kv + 1024 + s * 512 + (c & 1) * 256 + li * 16 + (c >> 1) * 8) = o;
        else {
            *(u32x2*)(kv + ((c & 1) * 32 + 16 * s + li) * 16 + (c >> 1) * 8) = o;
            const float n2 = rowmax16(xsum32(xsum16(dot2(o[0], o[0], dot2(o[1], o[1], 0.f)))));
            if (lane == 0) ((float*)(ws + KMAX_OFF))[g * 4 + sub] = n2;
        }
    }
    STAMP1(3);
}

constexpr unsigned L2_STAT = 131072, L2_ONES = 135168, L2_X = 139264, L2_W2 = 139264, L2_END = 163840;
DEVI u32x4 clean(u32x4 v) { return u32x4{min(v[0], 0xfffffffeu), min(v[1], 0xfffffffeu), min(v[2], 0xfffffffeu), min(v[3], 0xfffffffeu)}; }
template <int I> DEVI void poll_issue(u32x4& f, unsigned pend, rsrc_t rw, unsigned voff, unsigned soff) {
    asm volatile("s_bitcmp1_b32 %1, %5\n\ts_cbranch_scc0 .Lskip%=\n\tbuffer_load_dwordx4 %0, %2, %3, %4 offen sc1\n.Lskip%=:"
                 : "+v"(f) : "s"(pend), "v"(voff), "s"(rw), "s"(soff), "n"(I) : "memory", "scc");
}
DEVI void poll8(u32x4 (&f)[8], rsrc_t rw, unsigned voff, unsigned base, unsigned stride) {
    unsigned pend = 0xffu;
#pragma unroll
    for (int i = 0; i < 8; i++) f[i] = u32x4{0, 0, 0, 0};
    for (unsigned spins = 0; spins < (1u << 18); spins++) {
        asm volatile("s_nop 4" ::: "memory");
        poll_issue<0>(f[0], pend, rw, voff, base); poll_issue<1>(f[1], pend, rw, voff, base + stride);
        poll_issue<2>(f[2], pend, rw, voff, base + 2 * stride); poll_issue<3>(f[3], pend, rw, voff, base + 3 * stride);
        poll_issue<4>(f[4], pend, rw, voff, base + 4 * stride); poll_issue<5>(f[5], pend, rw, voff, base + 5 * stride);
        poll_issue<6>(f[6], pend, rw, voff, base + 6 * stride); poll_issue<7>(f[7], pend, rw, voff, base + 7 * stride);
        asm volatile("s_waitcnt vmcnt(0)" : "+v"(f[0]), "+v"(f[1]), "+v"(f[2]), "+v"(f[3]), "+v"(f[4]), "+v"(f[5]), "+v"(f[6]), "+v"(f[7]) :: "memory");
        unsigned np = 0;
#pragma unroll
        for (int i = 0; i < 8; i++) np |= __any(max(max(f[i][0], f[i][1]), max(f[i][2], f[i][3])) == ~0u) ? (1u << i) : 0u;
        pend = __builtin_amdgcn_readfirstlane(np);
        if (pend == 0) break;
    }
}

#define SLP 4
#define LD7 "s_nop 4\n\tbuffer_load_dwordx4 %0, %7, %8, %9 offen sc1\n\tbuffer_load_dwordx4 %1, %7, %8, %9 offen offset:1024 sc1\n\t"   \
            "buffer_load_dwordx4 %2, %7, %8, %9 offen offset:2048 sc1\n\tbuffer_load_dwordx4 %3, %7, %8, %9 offen offset:3072 sc1\n\t"    \
            "buffer_load_dwordx4 %4, %7, %8, %10 offen sc1\n\tbuffer_load_dwordx4 %5, %7, %8, %10 offen offset:1024 sc1\n\t"              \
            "buffer_load_dwordx4 %6, %7, %8, %10 offen offset:2048 sc1"
DEVI void issue7(u32x4 (&f)[7], rsrc_t rw, unsigned voff, unsigned soff) {
    asm volatile(LD7 : "=&v"(f[0]), "=&v"(f[1]), "=&v"(f[2]), "=&v"(f[3]), "=&v"(f[4]), "=&v"(f[5]), "=&v"(f[6])
                 : "v"(voff), "s"(rw), "s"(soff), "s"(soff + 4096) : "memory");
}
DEVI void issue7if(u32x4 (&f)[7], unsigned cond, rsrc_t rw, unsigned voff, unsigned soff) {
    asm volatile("s_cmp_eq_u32 %11, 0\n\ts_cbranch_scc1 .Lno7_%=\n\t" LD7 "\n.Lno7_%=:"
                 : "+v"(f[0]), "+v"(f[1]), "+v"(f[2]), "+v"(f[3]), "+v"(f[4]), "+v"(f[5]), "+v"(f[6])
                 : "v"(voff), "s"(rw), "s"(soff), "s"(soff + 4096), "s"(cond) : "memory", "scc");
}
DEVI void probe(rsrc_t rw, unsigned pv, unsigned soff) {
#pragma unroll 1
    for (unsigned spins = 0; spins < (1u << 18); spins++) {
        unsigned m;
        asm volatile("s_nop 4\n\tbuffer_load_dword %0, %1, %2, %3 offen sc1\n\ts_waitcnt vmcnt(0)" : "=&v"(m) : "v"(pv), "s"(rw), "s"(soff) : "memory");
        if (!__any(m == ~0u)) break;
        __builtin_amdgcn_s_sleep(SLP);
    }
}
template <int N> DEVI void check7(u32x4 (&f)[7], rsrc_t rw, unsigned voff, unsigned soff) {
    asm volatile("s_waitcnt vmcnt(%7)" : "+v"(f[0]), "+v"(f[1]), "+v"(f[2]), "+v"(f[3]), "+v"(f[4]), "+v"(f[5]), "+v"(f[6]) : "n"(N) : "memory");
#pragma unroll 1
    for (unsigned spins = 0; spins < 64; spins++) {
        unsigned m = 0;
#pragma unroll
        for (int i = 0; i < 7; i++) m = max(max(m, max(f[i][0], f[i][1])), max(f[i][2], f[i][3]));
        if (!__any(m == ~0u)) break;
        probe(rw, min(voff, 96u) * 64, soff);
        asm volatile(LD7 "\n\ts_waitcnt vmcnt(0)" : "+v"(f[0]), "+v"(f[1]), "+v"(f[2]), "+v"(f[3]), "+v"(f[4]), "+v"(f[5]), "+v"(f[6])
                     : "v"(voff), "s"(rw), "s"(soff), "s"(soff + 4096) : "memory");
    }
}

__global__ __launch_bounds__(512, 2) void k2_kernel(const unsigned short* __restrict__ x, const unsigned short* __restrict__ b1,
                                                    unsigned short* __restrict__ out, char* ws) {
    __shared__ __align__(16) char smem[L2_END];
    const unsigned sb = (unsigned)(size_t)(LDS3 char*)smem;
    const int tid = threadIdx.x, lane = tid & 63, w = __builtin_amdgcn_readfirstlane(tid >> 6);
    const int b = blockIdx.x, g = (b & 7) * 4 + (b >> 6), j = (b >> 3) & 7;
    const int r = lane & 31, h = lane >> 5, kh = w >> 2;
    const unsigned l16 = lane * 16;
    const rsrc_t rw = mkrsrc(ws, WS_END);
    STAMPDECL STAMP(0);
    u32x4 q0, q1; u32x2 km;
    asm volatile("s_nop 4\n\tbuffer_load_dwordx4 %0, %3, %4, %5 offen\n\tbuffer_load_dwordx4 %1, %3, %4, %6 offen\n\tbuffer_load_dwordx2 %2, %7, %4, 0 offen"
                 : "=&v"(q0), "=&v"(q1), "=&v"(km)
                 : "v"(l16), "s"(rw), "s"(Q_OFF + ((g * 2) * 32 + 4 * j + (w & 3)) * 1024), "s"(Q_OFF + ((g * 2 + 1) * 32 + 4 * j + (w & 3)) * 1024),
                   "v"(KMAX_OFF + lane * 8) : "memory");
#define KVCHUNK(off, n) _Pragma("unroll") for (int i = 0; i < n; i++) { const unsigned o_ = kh * 65536 + (off) + ((w & 3) * n + i) * 1024; dma16(rw, l16, KV_OFF + o_, sb + o_); }
#define WOPIECE(i) dma16(rw, l16, WO_OFF + (j * 64 + w * 8 + (i)) * 1024, sb + kh * 65536 + (w & 3) * 8192 + (i) * 1024);
    KVCHUNK(0, 2)
    BAR();
    KVCHUNK(8192, 4)
    if (tid < 256) sts128(sb + L2_ONES + tid * 16, u32x4{0x3f803f80u, 0x3f803f80u, 0x3f803f80u, 0x3f803f80u});
    asm volatile("s_waitcnt vmcnt(6)" : "+v"(q0), "+v"(q1), "+v"(km) :: "memory");
    float qn0 = 0.f, qn1 = 0.f;
#pragma unroll
    for (int e = 0; e < 4; e++) { qn0 = dot2(q0[e], q0[e], qn0); qn1 = dot2(q1[e], q1[e], qn1); }
    const float kmax2 = wavemax(fmaxf(__builtin_bit_cast(float, km[0]), __builtin_bit_cast(float, km[1])));
    const float mb = __builtin_sqrtf(fmaxf(xsum32(qn0), xsum32(qn1)) * kmax2) * 1.001f;
    const bool fast = __all(mb <= 60.f);
    const unsigned aslot = A_OFF + ((g * 8 + j) * 28 + 2 * (w < 7 ? w : 6)) * 1024;
    const rsrc_t ra = mkrsrc(ws, w < 7 ? WS_END : 0);
#define STA(v, soff) stc(v, ra, l16, soff)
#define MARK(i) if ((i) < 4) STA((u32x4{~0u, ~0u, ~0u, ~0u}), aslot + (((i) >> 1) * 14 + ((i) & 1)) * 1024); else STC((u32x4{~0u, ~0u, ~0u, ~0u}), l16, hslot);
    const int tt = w & 1, ct = (w >> 1) & 1;
    const unsigned hslot = H_OFF + (g * 64 + 8 * j + 4 * tt + 2 * ct + kh) * 1024;
    const unsigned w1o = W1_OFF + ((7 * j + (w < 7 ? w : 6)) * 32) * 1024;
    u32x4 w1f[32];
    f32x16 o0, o1;
    float m0, m1;
#pragma unroll
    for (int i = 0; i < 16; i++) { o0[i] = 0.f; o1[i] = 0.f; }
    unsigned ka = sb + kh * 65536 + l16, va = r < 16 ? sb + kh * 65536 + 1024 + h * 256 + r * 16 : sb + L2_ONES + ((r >= 20 && r < 28) ? 64 : 0);
    const unsigned vinc = r < 16 ? 2048 : 0;
    u32x4 warm = {0, 0, 0, 0};
#define TOUCH(i) asm volatile("s_nop 4\n\tbuffer_load_dwordx4 %0, %1, %2, %3 offen" : "+v"(warm) : "v"(l16), "s"(rw),                  \
        "s"((i) < 2 ? w1o + (24 + (b >> 6) * 2 + (i)) * 1024 : W2_OFF + (j * 224 + w * 28 + (b >> 6) * 7 + (i) - 2) * 1024) : "memory");
    if (fast) {
        f32x16 nm, sA, sB;
#pragma unroll
        for (int i = 0; i < 16; i++) nm[i] = -mb;
        m0 = mb; m1 = mb;
        u32x4 p1 = {0, 0, 0, 0}, k0, k1, va0, va1, vb0, vb1;
#define EY(S, i) e[i] = __builtin_amdgcn_exp2f(S[i]); SB;
#define CV(P, k, i) P[k] = cvtpk(e[i], e[i + 1]); SB;
#define STEP(S_, OP, VP1, SN, KN, QN, OC, VC0, LD) { float e[16]; u32x4 p0;                                                   \
        EY(S_, 0) EY(S_, 1) OP = MF32(B8(VP1), B8(p1), OP); SB; EY(S_, 2) EY(S_, 3) CV(p0, 0, 0) EY(S_, 4) EY(S_, 5) CV(p0, 1, 2) EY(S_, 6) \
        SN = MF32(B8(KN), B8(QN), nm); SB; LD EY(S_, 7) CV(p0, 2, 4) EY(S_, 8) EY(S_, 9) CV(p0, 3, 6) EY(S_, 10) EY(S_, 11)       \
        OC = MF32(B8(VC0), B8(p0), OC); SB; CV(p1, 0, 8) EY(S_, 12) EY(S_, 13) CV(p1, 1, 10) EY(S_, 14) EY(S_, 15) CV(p1, 2, 12) CV(p1, 3, 14) }
#define LDKV(K_, V0_, V1_, T) K_ = lds128(ka + (T) * 2048); V0_ = lds128(va); V1_ = lds128(va + 512); va += vinc; asm volatile("" : "+v"(va)); SB;
        WAITV(4); WAITL(); BAR();
        STAMP(11);
        LDKV(k0, va0, va1, 0)
        vb1 = va1;
        sA = MF32(B8(k0), B8(q0), nm);
#pragma unroll
        for (int it = 0; it < 16; it++) {
            if (it == 1) { WAITV(5); BAR(); }
            if (it == 5) { WAITV(14); BAR(); }
            if (it == 10) { WAITV(26); BAR(); }
            SB;
            STEP(sA, o1, vb1, sB, k0, q1, o0, va0, LDKV(k1, vb0, vb1, 2 * it + 1))
            if (it == 0) { KVCHUNK(24576, 5) } if (it == 1) { KVCHUNK(45056, 5) }
            if (it >= 2 && it < 14) { w1f[2 * it - 4] = LDW(l16, w1o + (2 * it - 4) * 1024); SB; }
            STEP(sB, o0, va1, sA, k1, q0, o1, va0, )
            if (it >= 2 && it < 7) { MARK(it - 2) SB; }
            if (it >= 10 && it < 14) { WOPIECE(2 * it - 20) WOPIECE(2 * it - 19) }
            STEP(sA, o1, va1, sB, k1, q1, o0, vb0, LDKV(k0, va0, va1, 2 * it + 2))
            if (it >= 2 && it < 14) { w1f[2 * it - 3] = LDW(l16, w1o + (2 * it - 3) * 1024); SB; }
            STEP(sB, o0, vb1, sA, k0, q0, o1, vb0, )
            if (it >= 5 && it < 14) { TOUCH(it - 5) SB; }
        }
        o1 = MF32(B8(vb1), B8(p1), o1);
    } else {
        KVCHUNK(24576, 5) KVCHUNK(45056, 5)
        WAITV(0); WAITL(); BAR();
        m0 = -3.0e38f; m1 = -3.0e38f;
        const f32x16 z = {0, 0, 0, 0, 0, 0, 0, 0, 0, 0, 0, 0, 0, 0, 0, 0};
#define SLOW(Q_, O_, M_) { f32x16 s = MF32(B8(kf), B8(Q_), z); float mx = s[0];                                               \
            _Pragma("unroll") for (int i = 1; i < 16; i++) mx = fmaxf(mx, s[i]);                                              \
            mx = fmaxf(xmax32(mx), M_); const float al = __builtin_amdgcn_exp2f(M_ - mx); M_ = mx; u32x4 p0, p1;              \
            _Pragma("unroll") for (int i = 0; i < 16; i++) O_[i] *= al;                                                       \
            _Pragma("unroll") for (int i = 0; i < 4; i++) {                                                                   \
                p0[i] = cvtpk(__builtin_amdgcn_exp2f(s[2 * i] - mx), __builtin_amdgcn_exp2f(s[2 * i + 1] - mx));              \
                p1[i] = cvtpk(__builtin_amdgcn_exp2f(s[2 * i + 8] - mx), __builtin_amdgcn_exp2f(s[2 * i + 9] - mx)); }        \
            O_ = MF32(B8(v0), B8(p0), O_); O_ = MF32(B8(v1), B8(p1), O_); }
#pragma unroll 1
        for (int t = 0; t < 32; t++) {
            if (t == 2 || t == 10 || t == 20) { WAITL(); BAR(); }
            if (t == 20) { _Pragma("unroll") for (int i = 0; i < 8; i++) WOPIECE(i) }
            const u32x4 kf = lds128(ka), v0 = lds128(va), v1 = lds128(va + 512);
            ka += 2048; va += vinc;
            SLOW(q0, o0, m0) SLOW(q1, o1, m1)
        }
#pragma unroll
        for (int i = 0; i < 24; i++) w1f[i] = LDW(l16, w1o + i * 1024);
#pragma unroll
        for (int i = 0; i < 5; i++) { MARK(i) }
    }
    STAMP(1);
    const f32x16 z16 = {0, 0, 0, 0, 0, 0, 0, 0, 0, 0, 0, 0, 0, 0, 0, 0};
    f32x16 cs = z16;
#define COLSUM(k0, k1) _Pragma("unroll") for (int ks = k0; ks < k1; ks++) cs = MF32(B8(w1f[ks]), B8((u32x4{0x3f803f80u, 0x3f803f80u, 0x3f803f80u, 0x3f803f80u})), cs); \
    asm volatile("" : "+v"(cs));
    {
        const f32x16 os = kh ? o0 : o1, ok = kh ? o1 : o0;
        const float ms = kh ? m0 : m1, mk = kh ? m1 : m0;
        const unsigned xa = sb + L2_X + w * 2560, xb = sb + L2_X + (w ^ 4) * 2560;
        *(LDS3 f32x4*)(size_t)(xa + l16) = f32x4{os[0], os[1], os[2], os[3]};
        *(LDS3 f32x4*)(size_t)(xa + 1024 + l16) = f32x4{os[4], os[5], os[6], os[7]};
        *(LDS3 f32x2*)(size_t)(xa + 2048 + lane * 8) = f32x2{os[8], ms};
        WAITV(0); WAITL(); BAR();
        asm volatile("" :: "v"(warm));
        const f32x4 a = *(LDS3 f32x4*)(size_t)(xb + l16), c = *(LDS3 f32x4*)(size_t)(xb + 1024 + l16);
        const f32x2 lm = *(LDS3 f32x2*)(size_t)(xb + 2048 + lane * 8);
        const float mm = fmaxf(mk, lm[1]), ea = __builtin_amdgcn_exp2f(mk - mm), eb = __builtin_amdgcn_exp2f(lm[1] - mm);
        const float inv = 1.f / (ok[8] * ea + lm[0] * eb), fa = ea * inv, fb = eb * inv;
        u32x4 of;
        of[0] = cvtpk(ok[0] * fa + a[0] * fb, ok[1] * fa + a[1] * fb); of[1] = cvtpk(ok[2] * fa + a[2] * fb, ok[3] * fa + a[3] * fb);
        of[2] = cvtpk(ok[4] * fa + c[0] * fb, ok[5] * fa + c[1] * fb); of[3] = cvtpk(ok[6] * fa + c[2] * fb, ok[7] * fa + c[3] * fb);
        STC(clean(of), l16, O_OFF + ((g * 2 + kh) * 32 + 4 * j + (w & 3)) * 1024);
    }
    STAMP(2);
    const unsigned xo = (64 * g + 32 * tt + r) * 512 + 64 * j + 32 * ct + 16 * kh;
    const u32x2 xa0 = *(const u32x2*)(x + xo + 4 * h), xa1 = *(const u32x2*)(x + xo + 8 + 4 * h);
    u32x2 bb[4];
#pragma unroll
    for (int i = 0; i < 4; i++) bb[i] = *(const u32x2*)(b1 + 224 * j + 32 * (w < 7 ? w : 6) + 8 * i + 4 * h);
    const unsigned myr = sb + kh * 65536 + 32768 + (w & 3) * 8192;
    const unsigned w2o = W2_OFF + (j * 224 + w * 28) * 1024;
    float hv[8];
    SB; COLSUM(0, 24) SB;
    {
        const int kq = w >> 1;
        u32x4 of[8], af[4];
        const unsigned wa = sb + kq * 8192 + l16;
        af[0] = lds128(wa); af[1] = lds128(wa + 65536); af[2] = lds128(wa + 1024); af[3] = lds128(wa + 65536 + 1024);
        poll8(of, rw, l16, O_OFF + ((g * 2 + tt) * 32 + 8 * kq) * 1024, 1024);
        u32x2 xb0 = xa0, xb1 = xa1;
        asm volatile("" : "+v"(xb0), "+v"(xb1), "+v"(bb[0]), "+v"(bb[1]), "+v"(bb[2]), "+v"(bb[3]));
        STAMP(3);
        f32x16 acc[2] = {z16, z16};
#pragma unroll
        for (int i = 0; i < 16; i++) {
            acc[i & 1] = MF32(B8(af[i & 3]), B8(of[i >> 1]), acc[i & 1]); SB;
            if (i < 12) { af[i & 3] = lds128(wa + (i & 1) * 65536 + ((i >> 1) + 2) * 1024); SB; }
            if (i & 1) { w1f[24 + (i >> 1)] = LDW(l16, w1o + (24 + (i >> 1)) * 1024); SB; }
        }
#pragma unroll
        for (int p = 0; p < 4; p++) {
            const unsigned xa = sb + (p >> 1) * 65536 + 32768 + (tt + 2 * (p & 1)) * 8192 + kq * 2048 + l16, o = (p >> 1) * 8;
            const f32x16 v = acc[p & 1];
            *(LDS3 f32x4*)(size_t)xa = f32x4{v[o], v[o + 1], v[o + 2], v[o + 3]};
            *(LDS3 f32x4*)(size_t)(xa + 1024) = f32x4{v[o + 4], v[o + 5], v[o + 6], v[o + 7]};
        }
        WAITL(); BAR();
        f32x4 a = {blo(xb0[0]), bhi(xb0[0]), blo(xb0[1]), bhi(xb0[1])}, c = {blo(xb1[0]), bhi(xb1[0]), blo(xb1[1]), bhi(xb1[1])};
#pragma unroll
        for (int p = 0; p < 4; p++) { a += *(LDS3 f32x4*)(size_t)(myr + p * 2048 + l16); c += *(LDS3 f32x4*)(size_t)(myr + p * 2048 + 1024 + l16); }
#pragma unroll
        for (int i = 0; i < 4; i++) { hv[i] = a[i]; hv[4 + i] = c[i]; }
        STC(clean(u32x4{cvtpk(hv[0], hv[1]), cvtpk(hv[2], hv[3]), cvtpk(hv[4], hv[5]), cvtpk(hv[6], hv[7])}), l16, hslot);
    }
    STAMP(4);
    SB; COLSUM(24, 32) SB;
#pragma unroll
    for (int i = 0; i < 4; i++) dma16(rw, l16, w2o + i * 1024, i < 8 ? myr + i * 1024 : sb + L2_W2 + (w * 3 + i - 8) * 1024);
    {
        u32x4 hf[8];
        poll8(hf, rw, l16, H_OFF + (g * 64 + 8 * w) * 1024, 1024);
        STAMP(5);
#pragma unroll
        for (int i = 0; i < 8; i++) sts128(sb + (i >> 2) * 65536 + (4 * w + (i & 3)) * 1024 + l16, hf[i]);
        f32x4 st;
#pragma unroll
        for (int t = 0; t < 2; t++) {
            float s0 = 0.f, q0_ = 0.f, s1 = 0.f, q1_ = 0.f;
#pragma unroll
            for (int i = 0; i < 4; i++)
#pragma unroll
                for (int e = 0; e < 4; e += 2) {
                    s0 = dot2(hf[4 * t + i][e], 0x3f803f80u, s0); q0_ = dot2(hf[4 * t + i][e], hf[4 * t + i][e], q0_);
                    s1 = dot2(hf[4 * t + i][e + 1], 0x3f803f80u, s1); q1_ = dot2(hf[4 * t + i][e + 1], hf[4 * t + i][e + 1], q1_);
                }
            st[2 * t] = xsum32(s0 + s1); st[2 * t + 1] = xsum32(q0_ + q1_);
        }
        if (h == 0) *(LDS3 f32x4*)(size_t)(sb + L2_STAT + w * 512 + r * 16) = st;
        if (tid == 0) *(LDS3 unsigned*)(size_t)(sb + L2_ONES) = 0;
        WAITL(); BAR();
    }
    STAMP(6);
    __builtin_amdgcn_s_setprio(1);
    u32x4 w2f[17], fa[7], fb[7];
    const unsigned ab = A_OFF + (g * 8 + w) * 28 * 1024;
    {
        f32x16 ac[2] = {z16, z16};
        u32x4 bf[4];
        unsigned ha0 = sb + l16, ha1 = sb + 65536 + l16;
        asm volatile("" : "+v"(ha1));
#pragma unroll
        for (int i = 0; i < 4; i++) bf[i] = lds128(((i & 1) ? ha1 : ha0) + (i >> 1) * 1024);
        f32x4 st = {0, 0, 0, 0};
        float mean[2], rstd[2];
        SB;
        sfor<64>([&](auto K) __attribute__((always_inline)) {
            constexpr int n = K.v;
            ac[n & 1] = MF32(B8(w1f[n >> 1]), B8(bf[n & 3]), ac[n & 1]); SB;
            if (n < 60) { bf[n & 3] = lds128(((n & 1) ? ha1 : ha0) + ((n >> 1) + 2) * 1024); SB; }
            if (n >= 4 && n < 12) { st += *(LDS3 f32x4*)(size_t)(sb + L2_STAT + (n - 4) * 512 + r * 16); SB; }
            if (n == 16) {
#pragma unroll
                for (int t = 0; t < 2; t++) {
                    mean[t] = st[2 * t] * (1.f / 512.f);
                    rstd[t] = __builtin_amdgcn_rsqf(fmaxf(st[2 * t + 1] * (1.f / 512.f) - mean[t] * mean[t], 0.f) + 1e-5f);
                }
                SB;
            }
            constexpr int m = (n >> 1) + 4;
            if ((n & 1) && m < 11) { dma16(rw, l16, w2o + m * 1024, m < 8 ? myr + m * 1024 : sb + L2_W2 + (w * 3 + m - 8) * 1024); SB; }
            if ((n & 1) && m >= 11 && m < 28) { w2f[m >= 11 && m < 28 ? m - 11 : 0] = LDW(l16, w2o + m * 1024); SB; }
        });
        STAMP(12);
#define PIN(x) asm volatile("" : "+v"(x));
        sfor<4>([&](auto K) __attribute__((always_inline)) {
            constexpr int t = K.v >> 1, s = K.v & 1;
            float z[8], u[8];
            u32x4 yp;
#define EL8 _Pragma("unroll") for (int i = 0; i < 8; i++)
            EL8 { u[i] = fmaf(-mean[t], cs[8 * s + i], ac[t][8 * s + i]); PIN(u[i]) }
            EL8 { const unsigned bw = bb[2 * s + (i >> 2)][(i >> 1) & 1]; z[i] = (i & 1) ? bhi(bw) : blo(bw); PIN(z[i]) }
            EL8 { z[i] = fmaf(u[i], rstd[t], z[i]); PIN(z[i]) }
            EL8 { u[i] = z[i] * LOG2E; PIN(u[i]) }
            EL8 { u[i] = __builtin_amdgcn_exp2f(u[i]); PIN(u[i]) }
            float a_[8];
            EL8 { a_[i] = u[i] + 2.f; PIN(a_[i]) }
            EL8 { u[i] = fmaf(u[i], a_[i], 2.f); PIN(u[i]) }
            EL8 { u[i] = __builtin_amdgcn_rcpf(u[i]); PIN(u[i]) }
            EL8 { u[i] = fmaf(u[i], -2.f, 1.f); PIN(u[i]) }
            EL8 { z[i] *= u[i]; PIN(z[i]) }
            EL8 if (i < 4) { yp[i & 3] = cvtpk(z[2 * (i & 3)], z[2 * (i & 3) + 1]); PIN(yp[i & 3]) }
            STA(clean(yp), aslot + (t * 14 + s) * 1024); SB;
            if (K.v == 1) {
                asm volatile("" : "+v"(w2f[0]), "+v"(w2f[1]), "+v"(w2f[2]), "+v"(w2f[3]), "+v"(w2f[4]), "+v"(w2f[5]), "+v"(w2f[6]), "+v"(w2f[7]), "+v"(w2f[8]),
                                  "+v"(w2f[9]), "+v"(w2f[10]), "+v"(w2f[11]), "+v"(w2f[12]), "+v"(w2f[13]), "+v"(w2f[14]), "+v"(w2f[15]), "+v"(w2f[16]));
#pragma unroll
                for (int i = 0; i < 7; i++) asm volatile("" : "=v"(fa[i]), "=v"(fb[i]));
                issue7if(fa, kh, rw, l16, ab); issue7if(fb, kh, rw, l16, ab + 14336); SB;
                STAMP(13);
            }
        });
    }
    STAMP(7);
    {
        u32x4 wl[2];
        __builtin_amdgcn_s_setprio(0);
        LDS3 unsigned* done = (LDS3 unsigned*)(size_t)(sb + L2_ONES);
        if (lane == 0) __hip_atomic_fetch_add(done, 1u, __ATOMIC_RELAXED, __HIP_MEMORY_SCOPE_WORKGROUP);
#define ALLDONE while (__builtin_amdgcn_readfirstlane(__hip_atomic_load(done, __ATOMIC_RELAXED, __HIP_MEMORY_SCOPE_WORKGROUP)) < 8) __builtin_amdgcn_s_sleep(2);
        const unsigned pv = (lane < 7 ? lane : lane < 14 ? lane + 7 : 0) * 1024;
        issue7if(fa, kh ^ 1, rw, l16, ab);
        if (!kh) probe(rw, min(l16, 96u) * 64, ab + 14336);
        issue7if(fb, kh ^ 1, rw, l16, ab + 14336);
        unsigned wla = myr + l16, wlb = sb + L2_W2 + w * 3072 + l16;
        asm volatile("" : "+v"(wla), "+v"(wlb));
#define WLD(n) lds128((n) < 8 ? wla + (n) * 1024 : wlb + ((n) - 8) * 1024)
#define F2(F, ks0, C0, C1) _Pragma("unroll") for (int k = 0; k < 7; k++) { const int n0 = 2 * ((ks0) + k), n1 = n0 + 1;               \
            C0 = MF32(B8(n0 < 11 ? wl[0] : w2f[n0 < 11 ? 0 : n0 - 11]), B8(F[k]), C0); SB; if (n0 + 2 < 11) { wl[0] = WLD(n0 + 2); SB; } \
            C1 = MF32(B8(n1 < 11 ? wl[1] : w2f[n1 < 11 ? 0 : n1 - 11]), B8(F[k]), C1); SB; if (n1 + 2 < 11) { wl[1] = WLD(n1 + 2); SB; } }
#define PST(v, t, cc, s) sts128(sb + (t) * 65536 + ((cc) + 2 * (s)) * 8192 + w * 1024 + l16,                                          \
            u32x4{cvtpk(v[8 * (s)], v[8 * (s) + 1]), cvtpk(v[8 * (s) + 2], v[8 * (s) + 3]), cvtpk(v[8 * (s) + 4], v[8 * (s) + 5]), cvtpk(v[8 * (s) + 6], v[8 * (s) + 7])});
        f32x16 c0 = z16, c1 = z16, c2 = z16, c3 = z16;
        wl[0] = WLD(0); wl[1] = WLD(1);
        check7<7>(fa, rw, l16, ab);
        STAMP(14);
        F2(fa, 0, c0, c1)
        issue7if(fa, kh, rw, l16, ab + 7168);
        wl[0] = WLD(0); wl[1] = WLD(1);
        if (!kh) WAITV(0);
        check7<7>(fb, rw, l16, ab + 14336);
        F2(fb, 0, c2, c3)
        if (!kh) { ALLDONE probe(rw, pv, ab + 7168); }
        issue7if(fa, kh ^ 1, rw, l16, ab + 7168);
        issue7(fb, rw, l16, ab + 21504);
        check7<7>(fa, rw, l16, ab + 7168);
        F2(fa, 7, c0, c1)
        if (kh) { ALLDONE asm volatile("" ::: "memory"); }
        PST(c0, 0, 0, 0) PST(c0, 0, 0, 1) PST(c1, 0, 1, 0) PST(c1, 0, 1, 1)
        check7<0>(fb, rw, l16, ab + 21504);
        STAMP(15);
        F2(fb, 7, c2, c3)
        STAMP(8);
        PST(c2, 1, 0, 0) PST(c2, 1, 0, 1) PST(c3, 1, 1, 0) PST(c3, 1, 1, 1)
        WAITL(); BAR();
        STAMP(9);
        unsigned sl = 0x00003f80u, sh = 0x3f800000u;
        asm volatile("" : "+s"(sl), "+s"(sh));
#pragma unroll
        for (int i = 0; i < 8; i++) {
            const u32x4 pf = lds128(sb + tt * 65536 + (w >> 1) * 8192 + i * 1024 + l16);
#pragma unroll
            for (int e = 0; e < 4; e++) { hv[2 * e] = dot2(pf[e], sl, hv[2 * e]); hv[2 * e + 1] = dot2(pf[e], sh, hv[2 * e + 1]); }
        }
        const u32x2 s0 = __builtin_amdgcn_permlane32_swap(cvtpk(hv[0], hv[1]), cvtpk(hv[4], hv[5]), false, false);
        const u32x2 s1 = __builtin_amdgcn_permlane32_swap(cvtpk(hv[2], hv[3]), cvtpk(hv[6], hv[7]), false, false);
        *(u32x4*)(out + xo + 8 * h) = u32x4{s0[0], s1[0], s0[1], s1[1]};
    }
    STAMP(10);
}

extern "C" void kernel_launch(void* const* d_in, const int* in_sizes, int n_in, void* d_out, int out_size, void* d_ws, size_t ws_size,
                              hipStream_t stream) {
    typedef const unsigned short* P;
    k1_kernel<<<256, 512, 0, stream>>>((P)d_in[0], (P)d_in[1], (P)d_in[2], (P)d_in[3], (P)d_in[4], (P)d_in[6], (P)d_in[8], (char*)d_ws);
    k2_kernel<<<256, 512, 0, stream>>>((P)d_in[0], (P)d_in[7], (unsigned short*)d_out, (char*)d_ws);
}
```

```cpp
#include <hip/hip_runtime.h>
#include <hip/hip_bf16.h>

typedef __attribute__((ext_vector_type(8))) __bf16 bf16x8;
typedef __attribute__((ext_vector_type(4))) __bf16 bf16x4;
typedef __attribute__((ext_vector_type(2))) __bf16 bf16x2;
typedef __attribute__((ext_vector_type(16))) float f32x16;
typedef __attribute__((ext_vector_type(4))) float f32x4;
typedef __attribute__((ext_vector_type(2))) float f32x2;
typedef __attribute__((ext_vector_type(4))) unsigned u32x4;
typedef __attribute__((ext_vector_type(2))) unsigned u32x2;
typedef __attribute__((ext_vector_type(4))) short s16x4;
typedef __amdgpu_buffer_rsrc_t rsrc_t;
#define LDS3 __attribute__((address_space(3)))
#define GLB1 __attribute__((address_space(1)))
#define DEVI __device__ __forceinline__
#define SB __builtin_amdgcn_sched_barrier(0)
#define B8(x) __builtin_bit_cast(bf16x8, x)
#define MF32(a, b, c) __builtin_amdgcn_mfma_f32_32x32x16_bf16(a, b, c, 0, 0, 0)
#define MF16(a, b, c) __builtin_amdgcn_mfma_f32_16x16x32_bf16(a, b, c, 0, 0, 0)

constexpr unsigned KMAX_OFF = 16384, KV_OFF = 32768, Q_OFF = 0x40000, WO_OFF = Q_OFF + (2u << 20),
                   W1_OFF = WO_OFF + (512u << 10), W2_OFF = W1_OFF + (1792u << 10), O_OFF = W2_OFF + (1792u << 10),
                   H_OFF = O_OFF + (2u << 20), A_OFF = H_OFF + (2u << 20), WS_END = A_OFF + (7u << 20);
constexpr unsigned K2_WARM = 26;
constexpr float LOG2E = 1.4426950408889634f;

template <int V> struct IC { static constexpr int v = V; };
template <int N, class F> DEVI void sfor(F&& f) { if constexpr (N > 0) { sfor<N - 1>(f); f(IC<N - 1>{}); } }
DEVI unsigned cvtpk(float a, float b) { f32x2 v = {a, b}; return __builtin_bit_cast(unsigned, __builtin_convertvector(v, bf16x2)); }
DEVI float blo(unsigned u) { return __builtin_bit_cast(float, u << 16); }
DEVI float bhi(unsigned u) { return __builtin_bit_cast(float, u & 0xffff0000u); }
DEVI rsrc_t mkrsrc(const void* p, unsigned bytes) { return __builtin_amdgcn_make_buffer_rsrc((void*)p, 0, bytes, 0x00020000); }
DEVI void dma16(rsrc_t rs, unsigned voff, unsigned soff, unsigned lds) {
    unsigned keep;
    asm volatile("s_mov_b32 %0, m0\n\ts_mov_b32 m0, %1\n\ts_nop 4\n\tbuffer_load_dwordx4 %2, %3, %4 offen lds\n\ts_mov_b32 m0, %0"
                 : "=&s"(keep) : "s"(lds), "v"(voff), "s"(rs), "s"(soff) : "memory");
}
#define WAITV(n) asm volatile("s_waitcnt vmcnt(" #n ")" ::: "memory")
#define WAITL() asm volatile("s_waitcnt lgkmcnt(0)" ::: "memory")
#define BAR() asm volatile("s_barrier" ::: "memory")
DEVI u32x4 lds128(unsigned a) { return *(LDS3 u32x4*)(size_t)a; }
DEVI void sts128(unsigned a, u32x4 v) { *(LDS3 u32x4*)(size_t)a = v; }
DEVI f32x2 swp32(float x) { float y = x; asm("s_nop 1\n\tv_permlane32_swap_b32 %0, %1\n\ts_nop 1" : "+v"(x), "+v"(y)); return f32x2{x, y}; }
DEVI f32x2 swp16(float x) { float y = x; asm("s_nop 1\n\tv_permlane16_swap_b32 %0, %1\n\ts_nop 1" : "+v"(x), "+v"(y)); return f32x2{x, y}; }
DEVI float xsum32(float x) { const f32x2 t = swp32(x); return t[0] + t[1]; }
DEVI float xmax32(float x) { const f32x2 t = swp32(x); return fmaxf(t[0], t[1]); }
DEVI float xsum16(float x) { const f32x2 t = swp16(x); return t[0] + t[1]; }
DEVI float xmax16(float x) { const f32x2 t = swp16(x); return fmaxf(t[0], t[1]); }
template <int C> DEVI float dppf(float x) { return __builtin_bit_cast(float, __builtin_amdgcn_update_dpp(0, __builtin_bit_cast(int, x), C, 0xf, 0xf, true)); }
DEVI float rowsum16(float x) { x += dppf<0xB1>(x); x += dppf<0x4E>(x); x += dppf<0x141>(x); x += dppf<0x140>(x); return x; }
DEVI float rowmax16(float x) { x = fmaxf(x, dppf<0xB1>(x)); x = fmaxf(x, dppf<0x4E>(x)); x = fmaxf(x, dppf<0x141>(x)); x = fmaxf(x, dppf<0x140>(x)); return x; }
DEVI float wavesum(float x) { return xsum32(xsum16(rowsum16(x))); }
DEVI float wavemax(float x) { return xmax32(xmax16(rowmax16(x))); }
DEVI float dot2(unsigned a, unsigned b, float c) { return __builtin_amdgcn_fdot2_f32_bf16(__builtin_bit_cast(bf16x2, a), __builtin_bit_cast(bf16x2, b), c, false); }

#define LDW(voff, soff) __builtin_amdgcn_raw_buffer_load_b128(rw, voff, soff, 0)
#define LDC(voff, soff) __builtin_amdgcn_raw_buffer_load_b128(rw, voff, soff, 16)
DEVI void stc(u32x4 v, rsrc_t rs, unsigned voff, unsigned soff) { __builtin_amdgcn_raw_buffer_store_b128(v, rs, voff, soff, 16); asm volatile("s_nop 1" :: "v"(v) : "memory"); }
#define STC(v, voff, soff) stc(v, rw, voff, soff)
#define CODEADDR(pc, sym) asm volatile("s_getpc_b64 vcc\n\ts_add_u32 vcc_lo, vcc_lo, " sym "@rel32@lo+4\n\t"                       \
        "s_addc_u32 vcc_hi, vcc_hi, " sym "@rel32@hi+12\n\ts_mov_b64 %0, vcc" : "=s"(pc) :: "vcc", "scc")
#define K2SYM "_Z9k2_kernelPKtS0_PtPc"
constexpr unsigned L1_X = 0, L1_WQ = 65536, L1_WKV = 131072, L1_PK = 147456, L1_END = 163840;
DEVI bf16x8 ldtr(unsigned a, unsigned b) {
    const s16x4 lo = __builtin_amdgcn_ds_read_tr16_b64_v4i16((LDS3 s16x4*)(size_t)a), hi = __builtin_amdgcn_ds_read_tr16_b64_v4i16((LDS3 s16x4*)(size_t)b);
    return B8(__builtin_shufflevector(lo, hi, 0, 1, 2, 3, 4, 5, 6, 7));
}
__global__ __launch_bounds__(512, 2) void k1_kernel(const unsigned short* __restrict__ x, const unsigned short* __restrict__ wq,
        const unsigned short* __restrict__ wk, const unsigned short* __restrict__ wv, const unsigned short* __restrict__ wo,
        const unsigned short* __restrict__ w1, const unsigned short* __restrict__ w2, char* __restrict__ ws) {
    __shared__ __align__(16) char smem[L1_END];
    const unsigned sb = (unsigned)(size_t)(LDS3 char*)smem;
    const int tid = threadIdx.x, lane = tid & 63, w = __builtin_amdgcn_readfirstlane(tid >> 6);
    const int b = blockIdx.x, g = (b & 7) * 4 + (b >> 6), j = (b >> 3) & 7;
    const int rl = lane >> 3, pl = lane & 7;
    const rsrc_t rx = mkrsrc(x, 2048 * 512 * 2), rq = mkrsrc(wq, 512 * 512 * 2), rkv = mkrsrc((j & 1) ? wv : wk, 512 * 16 * 2);
    u32x4 xr[8];
    {
        const unsigned vo = rl * 1024 + pl * 16, so = (g * 64 + w * 8) * 1024;
        asm volatile("s_nop 4\n\tbuffer_load_dwordx4 %0, %8, %9, %10 offen\n\tbuffer_load_dwordx4 %1, %8, %9, %10 offen offset:128\n\t"
                     "buffer_load_dwordx4 %2, %8, %9, %10 offen offset:256\n\tbuffer_load_dwordx4 %3, %8, %9, %10 offen offset:384\n\t"
                     "buffer_load_dwordx4 %4, %8, %9, %10 offen offset:512\n\tbuffer_load_dwordx4 %5, %8, %9, %10 offen offset:640\n\t"
                     "buffer_load_dwordx4 %6, %8, %9, %10 offen offset:768\n\tbuffer_load_dwordx4 %7, %8, %9, %10 offen offset:896"
                     : "=&v"(xr[0]), "=&v"(xr[1]), "=&v"(xr[2]), "=&v"(xr[3]), "=&v"(xr[4]), "=&v"(xr[5]), "=&v"(xr[6]), "=&v"(xr[7])
                     : "v"(vo), "s"(rx), "s"(so) : "memory");
    }
#pragma unroll
    for (int i = 0; i < 8; i++)
        dma16(rq, rl * 1024 + (((pl >> 1) ^ (((rl >> 1) & 1) | ((i & 1) << 1))) * 32) + (pl & 1) * 16, (64 * w + 8 * i) * 1024 + j * 128, sb + L1_WQ + (w * 8 + i) * 1024);
#pragma unroll
    for (int i = 0; i < 2; i++) dma16(rkv, lane * 16, (w * 2 + i) * 1024, sb + L1_WKV + (w * 2 + i) * 1024);
    asm volatile("s_waitcnt vmcnt(10)" : "+v"(xr[0]), "+v"(xr[1]), "+v"(xr[2]), "+v"(xr[3]), "+v"(xr[4]), "+v"(xr[5]), "+v"(xr[6]), "+v"(xr[7]) :: "memory");
    {
        float s0 = 0.f, s1 = 0.f, q0 = 0.f, q1 = 0.f;
#pragma unroll
        for (int i = 0; i < 8; i++)
#pragma unroll
            for (int e = 0; e < 4; e += 2) {
                s0 = dot2(xr[i][e], 0x3f803f80u, s0); q0 = dot2(xr[i][e], xr[i][e], q0);
                s1 = dot2(xr[i][e + 1], 0x3f803f80u, s1); q1 = dot2(xr[i][e + 1], xr[i][e + 1], q1);
            }
        float s = s0 + s1, q = q0 + q1;
        s += dppf<0xB1>(s); q += dppf<0xB1>(q); s += dppf<0x4E>(s); q += dppf<0x4E>(q); s += dppf<0x141>(s); q += dppf<0x141>(q);
        s *= (1.f / 512.f); q *= (1.f / 512.f);
        const float rstd = __builtin_amdgcn_rsqf(fmaxf(q - s * s, 0.f) + 1e-5f), nm = -s * rstd;
        const int row = w * 8 + rl;
#pragma unroll
        for (int i = 0; i < 8; i++) {
            u32x4 o;
#pragma unroll
            for (int e = 0; e < 4; e++) o[e] = cvtpk(fmaf(blo(xr[i][e]), rstd, nm), fmaf(bhi(xr[i][e]), rstd, nm));
            sts128(sb + L1_X + row * 1024 + (((8 * i + pl) ^ (row & 15)) * 16), o);
        }
    }
    unsigned pd0, pd1;
    {
        const int id = b * 8 + w;
        const unsigned short* src; int ld, k0, n0; unsigned bytes;
        if (id < 256)       { src = wo; ld = 512;  bytes = 512 * 512 * 2;  k0 = (id & 31) * 16; n0 = (id >> 5) * 64; pd0 = WO_OFF + ((id >> 5) * 64 + (id & 31)) * 1024; pd1 = pd0 + 32768; }
        else if (id < 1152) { const int i2 = id - 256; src = w1; ld = 1792; bytes = 512 * 1792 * 2; k0 = (i2 & 31) * 16; n0 = (i2 >> 5) * 64;
                              pd0 = W1_OFF + ((i2 >> 5) * 64 + (i2 & 31)) * 1024; pd1 = pd0 + 32768; }
        else                { const int i2 = id - 1152, jc = i2 / 112, fk = i2 - jc * 112;
                              src = w2; ld = 512; bytes = 512 * 1792 * 2; k0 = fk * 16; n0 = jc * 64; pd0 = W2_OFF + i2 * 2048; pd1 = pd0 + 1024; }
        const rsrc_t rp = mkrsrc(src, bytes);
#pragma unroll
        for (int i = 0; i < 2; i++) dma16(rp, rl * ld * 2 + pl * 16, ((k0 + 8 * i) * ld + n0) * 2, sb + L1_PK + w * 2048 + i * 1024);
    }
    WAITV(2); WAITL(); BAR();
    u32x4 warm;
    {
        unsigned long long pc;
        CODEADDR(pc, K2SYM);
        const unsigned piece = ((b >> 3) * 8 + w) % K2_WARM;
        asm volatile("s_nop 4\n\tglobal_load_dwordx4 %0, %1, %2" : "=&v"(warm) : "v"(piece * 1024 + lane * 16), "s"(pc) : "memory");
    }
    const int c = lane >> 4, li = lane & 15, hq = w & 3, tt = w >> 2;
    const rsrc_t rw = mkrsrc(ws, WS_END);
    STC((u32x4{~0u, ~0u, ~0u, ~0u}), lane * 16, O_OFF + (b * 8 + w) * 1024);
    const unsigned arow = 8 * c + (li >> 2);
    const unsigned abase = sb + L1_WQ + arow * 128 + ((hq ^ (((li >> 3) & 1) | ((c & 1) << 1))) * 32) + (li & 3) * 8;
    unsigned bb[4];
#pragma unroll
    for (int m = 0; m < 4; m++) bb[m] = sb + L1_X + li * 1024 + (((4 * m + c) ^ li) * 16);
    const int sub = j >> 1;
    {
        const unsigned wb = sb + L1_WKV + arow * 32 + (li & 3) * 8 + w * 2048;
        const unsigned xb = sb + L1_X + (sub * 16 + li) * 1024 + (w >> 1) * 256;
        f32x4 d = {0, 0, 0, 0};
#pragma unroll
        for (int u = 0; u < 2; u++) {
            const bf16x8 wf = ldtr(wb + u * 1024, wb + u * 1024 + 128);
            const bf16x8 xf = B8(lds128(xb + (((8 * (w & 1) + 4 * u + c) ^ li) * 16)));
            if (j & 1) d = MF16(xf, wf, d); else d = MF16(wf, xf, d);
        }
        *(LDS3 f32x4*)(size_t)(sb + L1_WKV + w * 2048 + lane * 16) = d;
    }
    f32x4 a0 = {0, 0, 0, 0}, a1 = {0, 0, 0, 0};
    {
        bf16x8 af[3]; u32x4 b0[3], b1[3];
#define K1LD(ks) { af[(ks) % 3] = ldtr(abase + (ks) * 4096, abase + (ks) * 4096 + 512); const unsigned ba = bb[(ks) & 3] + tt * 32768 + ((ks) >> 2) * 256; \
                   b0[(ks) % 3] = lds128(ba); b1[(ks) % 3] = lds128(ba + 16384); SB; }
        K1LD(0) K1LD(1) K1LD(2)
#pragma unroll
        for (int ks = 0; ks < 16; ks++) {
            a0 = MF16(af[ks % 3], B8(b0[ks % 3]), a0); a1 = MF16(af[ks % 3], B8(b1[ks % 3]), a1); SB;
            if (ks < 13) K1LD(ks + 3)
        }
    }
    {
        const float sc = 0.25f * LOG2E;
        const u32x2 s0 = __builtin_amdgcn_permlane32_swap(cvtpk(a0[0] * sc, a0[1] * sc), cvtpk(a1[0] * sc, a1[1] * sc), false, false);
        const u32x2 s1 = __builtin_amdgcn_permlane32_swap(cvtpk(a0[2] * sc, a0[3] * sc), cvtpk(a1[2] * sc, a1[3] * sc), false, false);
        STC((u32x4{s0[0], s1[0], s0[1], s1[1]}), ((c & 1) * 32 + (c >> 1) * 16 + li) * 16, Q_OFF + ((g * 2 + tt) * 32 + 4 * j + hq) * 1024);
        WAITV(2);
        asm volatile("" :: "v"(warm));
        const unsigned pa = sb + L1_PK + w * 2048 + (4 * (c >> 1) + (li >> 2)) * 128 + (c & 1) * 32 + (li & 3) * 8;
        STC(__builtin_bit_cast(u32x4, ldtr(pa, pa + 1024)), lane * 16, pd0);
        STC(__builtin_bit_cast(u32x4, ldtr(pa + 64, pa + 1088)), lane * 16, pd1);
    }
    WAITL(); BAR();
    if (w == 0) {
        f32x4 d = {0, 0, 0, 0};
#pragma unroll
        for (int i = 0; i < 8; i++) d += *(LDS3 f32x4*)(size_t)(sb + L1_WKV + i * 2048 + lane * 16);
        const u32x2 o = {cvtpk(d[0], d[1]), cvtpk(d[2], d[3])};
        const int s = sub & 1;
        char* kv = ws + KV_OFF + (g * 2 + (sub >> 1)) * 2048;
        if (j & 1) *(u32x2*)(kv + 1024 + s * 512 + (c & 1) * 256 + li * 16 + (c >> 1) * 8) = o;
        else {
            *(u32x2*)(kv + ((c & 1) * 32 + 16 * s + li) * 16 + (c >> 1) * 8) = o;
            const float n2 = rowmax16(xsum32(xsum16(dot2(o[0], o[0], dot2(o[1], o[1], 0.f)))));
            if (lane == 0) ((float*)(ws + KMAX_OFF))[g * 4 + sub] = n2;
        }
    }
}

constexpr unsigned L2_STAT = 131072, L2_ONES = 135168, L2_X = 139264, L2_W2 = 139264, L2_END = 163840;
DEVI u32x4 clean(u32x4 v) { return u32x4{min(v[0], 0xfffffffeu), min(v[1], 0xfffffffeu), min(v[2], 0xfffffffeu), min(v[3], 0xfffffffeu)}; }
template <int I> DEVI void poll_issue(u32x4& f, unsigned pend, rsrc_t rw, unsigned voff, unsigned soff) {
    asm volatile("s_bitcmp1_b32 %1, %5\n\ts_cbranch_scc0 .Lskip%=\n\tbuffer_load_dwordx4 %0, %2, %3, %4 offen sc1\n.Lskip%=:"
                 : "+v"(f) : "s"(pend), "v"(voff), "s"(rw), "s"(soff), "n"(I) : "memory", "scc");
}
DEVI void poll8(u32x4 (&f)[8], rsrc_t rw, unsigned voff, unsigned base, unsigned stride) {
    unsigned pend = 0xffu;
#pragma unroll
    for (int i = 0; i < 8; i++) f[i] = u32x4{0, 0, 0, 0};
    for (unsigned spins = 0; spins < (1u << 18); spins++) {
        asm volatile("s_nop 4" ::: "memory");
        poll_issue<0>(f[0], pend, rw, voff, base); poll_issue<1>(f[1], pend, rw, voff, base + stride);
        poll_issue<2>(f[2], pend, rw, voff, base + 2 * stride); poll_issue<3>(f[3], pend, rw, voff, base + 3 * stride);
        poll_issue<4>(f[4], pend, rw, voff, base + 4 * stride); poll_issue<5>(f[5], pend, rw, voff, base + 5 * stride);
        poll_issue<6>(f[6], pend, rw, voff, base + 6 * stride); poll_issue<7>(f[7], pend, rw, voff, base + 7 * stride);
        asm volatile("s_waitcnt vmcnt(0)" : "+v"(f[0]), "+v"(f[1]), "+v"(f[2]), "+v"(f[3]), "+v"(f[4]), "+v"(f[5]), "+v"(f[6]), "+v"(f[7]) :: "memory");
        unsigned np = 0;
#pragma unroll
        for (int i = 0; i < 8; i++) np |= __any(max(max(f[i][0], f[i][1]), max(f[i][2], f[i][3])) == ~0u) ? (1u << i) : 0u;
        pend = __builtin_amdgcn_readfirstlane(np);
        if (pend == 0) break;
    }
}

#define LD7 "s_nop 4\n\tbuffer_load_dwordx4 %0, %7, %8, %9 offen sc1\n\tbuffer_load_dwordx4 %1, %7, %8, %9 offen offset:1024 sc1\n\t"   \
            "buffer_load_dwordx4 %2, %7, %8, %9 offen offset:2048 sc1\n\tbuffer_load_dwordx4 %3, %7, %8, %9 offen offset:3072 sc1\n\t"    \
            "buffer_load_dwordx4 %4, %7, %8, %10 offen sc1\n\tbuffer_load_dwordx4 %5, %7, %8, %10 offen offset:1024 sc1\n\t"              \
            "buffer_load_dwordx4 %6, %7, %8, %10 offen offset:2048 sc1"
DEVI void issue7(u32x4 (&f)[7], rsrc_t rw, unsigned voff, unsigned soff) {
    asm volatile(LD7 : "=&v"(f[0]), "=&v"(f[1]), "=&v"(f[2]), "=&v"(f[3]), "=&v"(f[4]), "=&v"(f[5]), "=&v"(f[6])
                 : "v"(voff), "s"(rw), "s"(soff), "s"(soff + 4096) : "memory");
}
DEVI void issue7if(u32x4 (&f)[7], unsigned cond, rsrc_t rw, unsigned voff, unsigned soff) {
    asm volatile("s_cmp_eq_u32 %11, 0\n\ts_cbranch_scc1 .Lno7_%=\n\t" LD7 "\n.Lno7_%=:"
                 : "+v"(f[0]), "+v"(f[1]), "+v"(f[2]), "+v"(f[3]), "+v"(f[4]), "+v"(f[5]), "+v"(f[6])
                 : "v"(voff), "s"(rw), "s"(soff), "s"(soff + 4096), "s"(cond) : "memory", "scc");
}
DEVI void probe(rsrc_t rw, unsigned pv, unsigned soff) {
#pragma unroll 1
    for (unsigned spins = 0; spins < (1u << 18); spins++) {
        unsigned m;
        asm volatile("s_nop 4\n\tbuffer_load_dword %0, %1, %2, %3 offen sc1\n\ts_waitcnt vmcnt(0)" : "=&v"(m) : "v"(pv), "s"(rw), "s"(soff) : "memory");
        if (!__any(m == ~0u)) break;
        __builtin_amdgcn_s_sleep(4);
    }
}
template <int N> DEVI void check7(u32x4 (&f)[7], rsrc_t rw, unsigned voff, unsigned soff) {
    asm volatile("s_waitcnt vmcnt(%7)" : "+v"(f[0]), "+v"(f[1]), "+v"(f[2]), "+v"(f[3]), "+v"(f[4]), "+v"(f[5]), "+v"(f[6]) : "n"(N) : "memory");
#pragma unroll 1
    for (unsigned spins = 0; spins < 64; spins++) {
        unsigned m = 0;
#pragma unroll
        for (int i = 0; i < 7; i++) m = max(max(m, max(f[i][0], f[i][1])), max(f[i][2], f[i][3]));
        if (!__any(m == ~0u)) break;
        probe(rw, min(voff, 96u) * 64, soff);
        asm volatile(LD7 "\n\ts_waitcnt vmcnt(0)" : "+v"(f[0]), "+v"(f[1]), "+v"(f[2]), "+v"(f[3]), "+v"(f[4]), "+v"(f[5]), "+v"(f[6])
                     : "v"(voff), "s"(rw), "s"(soff), "s"(soff + 4096) : "memory");
    }
}

__global__ __launch_bounds__(512, 2) void k2_kernel(const unsigned short* __restrict__ x, const unsigned short* __restrict__ b1,
                                                    unsigned short* __restrict__ out, char* ws) {
    __shared__ __align__(16) char smem[L2_END];
    const unsigned sb = (unsigned)(size_t)(LDS3 char*)smem;
    const int tid = threadIdx.x, lane = tid & 63, w = __builtin_amdgcn_readfirstlane(tid >> 6);
    const int b = blockIdx.x, g = (b & 7) * 4 + (b >> 6), j = (b >> 3) & 7;
    const int r = lane & 31, h = lane >> 5, kh = w >> 2;
    const unsigned l16 = lane * 16;
    const rsrc_t rw = mkrsrc(ws, WS_END);
    u32x4 q0, q1; u32x2 km;
    asm volatile("s_nop 4\n\tbuffer_load_dwordx4 %0, %3, %4, %5 offen\n\tbuffer_load_dwordx4 %1, %3, %4, %6 offen\n\tbuffer_load_dwordx2 %2, %7, %4, 0 offen"
                 : "=&v"(q0), "=&v"(q1), "=&v"(km)
                 : "v"(l16), "s"(rw), "s"(Q_OFF + ((g * 2) * 32 + 4 * j + (w & 3)) * 1024), "s"(Q_OFF + ((g * 2 + 1) * 32 + 4 * j + (w & 3)) * 1024),
                   "v"(KMAX_OFF + lane * 8) : "memory");
#define KVCHUNK(off, n) _Pragma("unroll") for (int i = 0; i < n; i++) { const unsigned o_ = kh * 65536 + (off) + ((w & 3) * n + i) * 1024; dma16(rw, l16, KV_OFF + o_, sb + o_); }
#define WOPIECE(i) dma16(rw, l16, WO_OFF + (j * 64 + w * 8 + (i)) * 1024, sb + kh * 65536 + (w & 3) * 8192 + (i) * 1024);
    KVCHUNK(0, 2)
    BAR();
    KVCHUNK(8192, 4)
    if (tid < 256) { const unsigned o1_ = (tid & 3) ? 0u : 0x3f803f80u; sts128(sb + L2_ONES + tid * 16, u32x4{o1_, o1_, o1_, o1_}); }
    asm volatile("s_waitcnt vmcnt(6)" : "+v"(q0), "+v"(q1), "+v"(km) :: "memory");
    float qn0 = 0.f, qn1 = 0.f;
#pragma unroll
    for (int e = 0; e < 4; e++) { qn0 = dot2(q0[e], q0[e], qn0); qn1 = dot2(q1[e], q1[e], qn1); }
    const float kmax2 = wavemax(fmaxf(__builtin_bit_cast(float, km[0]), __builtin_bit_cast(float, km[1])));
    const float mb = __builtin_sqrtf(fmaxf(xsum32(qn0), xsum32(qn1)) * kmax2) * 1.001f;
    const bool fast = __all(mb <= 60.f);
    const unsigned aslot = A_OFF + ((g * 8 + j) * 28 + 2 * (w < 7 ? w : 6)) * 1024;
    const rsrc_t ra = mkrsrc(ws, w < 7 ? WS_END : 0);
#define STA(v, soff) stc(v, ra, l16, soff)
#define MARK(i) if ((i) < 4) STA((u32x4{~0u, ~0u, ~0u, ~0u}), aslot + (((i) >> 1) * 14 + ((i) & 1)) * 1024); else STC((u32x4{~0u, ~0u, ~0u, ~0u}), l16, hslot);
    const int tt = w & 1, ct = (w >> 1) & 1;
    const unsigned hslot = H_OFF + (g * 64 + 8 * j + 4 * tt + 2 * ct + kh) * 1024;
    const unsigned w1o = W1_OFF + ((7 * j + (w < 7 ? w : 6)) * 32) * 1024;
#define LDA(soff) __builtin_amdgcn_raw_buffer_load_b128(ra, l16, soff, 0)
    u32x4 w1f[32];
    f32x16 o0, o1;
    float m0, m1;
#pragma unroll
    for (int i = 0; i < 16; i++) { o0[i] = 0.f; o1[i] = 0.f; }
    unsigned ka = sb + kh * 65536 + l16, va = r < 16 ? sb + kh * 65536 + 1024 + h * 256 + r * 16 : sb + L2_ONES + ((r >= 20 && r < 28) ? 64 : 0) + ((r & 27) == 16 ? 0 : 16);
    const unsigned vinc = r < 16 ? 2048 : 0;
    u32x4 warm = {0, 0, 0, 0};
#define TOUCH(i) asm volatile("s_nop 4\n\tbuffer_load_dwordx4 %0, %1, %2, %3 offen" : "+v"(warm) : "v"(l16), "s"(rw),                  \
        "s"((i) < 2 ? w1o + (24 + (b >> 6) * 2 + (i)) * 1024 : W2_OFF + (j * 224 + w * 28 + (b >> 6) * 7 + (i) - 2) * 1024) : "memory");
    if (fast) {
        f32x16 nm, sA, sB;
#pragma unroll
        for (int i = 0; i < 16; i++) nm[i] = -mb;
        m0 = mb; m1 = mb;
        u32x4 p1 = {0, 0, 0, 0}, k0, k1, va0, va1, vb0, vb1;
#define EY(S, i) e[i] = __builtin_amdgcn_exp2f(S[i]); SB;
#define CV(P, k, i) P[k] = cvtpk(e[i], e[i + 1]); SB;
#define STEP(S_, OP, VP1, SN, KN, QN, OC, VC0, LD) { float e[16]; u32x4 p0;                                                   \
        EY(S_, 0) EY(S_, 1) OP = MF32(B8(VP1), B8(p1), OP); SB; EY(S_, 2) EY(S_, 3) CV(p0, 0, 0) EY(S_, 4) EY(S_, 5) CV(p0, 1, 2) EY(S_, 6) \
        SN = MF32(B8(KN), B8(QN), nm); SB; LD EY(S_, 7) CV(p0, 2, 4) EY(S_, 8) EY(S_, 9) CV(p0, 3, 6) EY(S_, 10) EY(S_, 11)       \
        OC = MF32(B8(VC0), B8(p0), OC); SB; CV(p1, 0, 8) EY(S_, 12) EY(S_, 13) CV(p1, 1, 10) EY(S_, 14) EY(S_, 15) CV(p1, 2, 12) CV(p1, 3, 14) }
#define LDKV(K_, V0_, V1_, T) K_ = lds128(ka + (T) * 2048); V0_ = lds128(va); V1_ = lds128(va + 512); va += vinc; asm volatile("" : "+v"(va)); SB;
#define PRIO(it) switch ("103.2..1.03..2..1..03...2...1..0"[it]) { case '3': __builtin_amdgcn_s_setprio(3); break; case '2': __builtin_amdgcn_s_setprio(2); break;  \
                                                      case '1': __builtin_amdgcn_s_setprio(1); break; case '0': __builtin_amdgcn_s_setprio(0); break; default: break; }
        WAITV(4); WAITL(); BAR();
        LDKV(k0, va0, va1, 0)
        vb1 = va1;
        sA = MF32(B8(k0), B8(q0), nm);
#pragma unroll
        for (int it = 0; it < 16; it++) {
            if (it == 1) { WAITV(5); BAR(); }
            if (it == 5) { WAITV(14); BAR(); }
            if (it == 10) { WAITV(26); BAR(); }
            PRIO(2 * it)
            SB;
            STEP(sA, o1, vb1, sB, k0, q1, o0, va0, LDKV(k1, vb0, vb1, 2 * it + 1))
            if (it == 0) { KVCHUNK(24576, 5) } if (it == 1) { KVCHUNK(45056, 5) }
            if (it >= 2 && it < 14) { w1f[2 * it - 4] = LDA(w1o + (2 * it - 4) * 1024); SB; }
            STEP(sB, o0, va1, sA, k1, q0, o1, va0, )
            if (it >= 2 && it < 7) { MARK(it - 2) SB; }
            if (it >= 10 && it < 14) { WOPIECE(2 * it - 20) WOPIECE(2 * it - 19) }
            PRIO(2 * it + 1) SB;
            STEP(sA, o1, va1, sB, k1, q1, o0, vb0, LDKV(k0, va0, va1, 2 * it + 2))
            if (it >= 2 && it < 14) { w1f[2 * it - 3] = LDA(w1o + (2 * it - 3) * 1024); SB; }
            STEP(sB, o0, vb1, sA, k0, q0, o1, vb0, )
            if (it >= 5 && it < 14) { TOUCH(it - 5) SB; }
        }
        o1 = MF32(B8(vb1), B8(p1), o1);
    } else {
        KVCHUNK(24576, 5) KVCHUNK(45056, 5)
        WAITV(0); WAITL(); BAR();
        m0 = -3.0e38f; m1 = -3.0e38f;
        const f32x16 z = {0, 0, 0, 0, 0, 0, 0, 0, 0, 0, 0, 0, 0, 0, 0, 0};
#define SLOW(Q_, O_, M_) { f32x16 s = MF32(B8(kf), B8(Q_), z); float mx = s[0];                                               \
            _Pragma("unroll") for (int i = 1; i < 16; i++) mx = fmaxf(mx, s[i]);                                              \
            mx = fmaxf(xmax32(mx), M_); const float al = __builtin_amdgcn_exp2f(M_ - mx); M_ = mx; u32x4 p0, p1;              \
            _Pragma("unroll") for (int i = 0; i < 16; i++) O_[i] *= al;                                                       \
            _Pragma("unroll") for (int i = 0; i < 4; i++) {                                                                   \
                p0[i] = cvtpk(__builtin_amdgcn_exp2f(s[2 * i] - mx), __builtin_amdgcn_exp2f(s[2 * i + 1] - mx));              \
                p1[i] = cvtpk(__builtin_amdgcn_exp2f(s[2 * i + 8] - mx), __builtin_amdgcn_exp2f(s[2 * i + 9] - mx)); }        \
            O_ = MF32(B8(v0), B8(p0), O_); O_ = MF32(B8(v1), B8(p1), O_); }
#pragma unroll 1
        for (int t = 0; t < 32; t++) {
            if (t == 2 || t == 10 || t == 20) { WAITL(); BAR(); }
            if (t == 20) { _Pragma("unroll") for (int i = 0; i < 8; i++) WOPIECE(i) }
            const u32x4 kf = lds128(ka), v0 = lds128(va), v1 = lds128(va + 512);
            ka += 2048; va += vinc;
            SLOW(q0, o0, m0) SLOW(q1, o1, m1)
        }
#pragma unroll
        for (int i = 0; i < 24; i++) w1f[i] = LDA(w1o + i * 1024);
#pragma unroll
        for (int i = 0; i < 5; i++) { MARK(i) }
    }
    const f32x16 z16 = {0, 0, 0, 0, 0, 0, 0, 0, 0, 0, 0, 0, 0, 0, 0, 0};
    f32x16 cs = z16;
#define COLSUM(k0, k1) _Pragma("unroll") for (int ks = k0; ks < k1; ks++) cs = MF32(B8(w1f[ks]), B8((u32x4{0x3f803f80u, 0x3f803f80u, 0x3f803f80u, 0x3f803f80u})), cs); \
    asm volatile("" : "+v"(cs));
    {
        const f32x16 os = kh ? o0 : o1, ok = kh ? o1 : o0;
        const float ms = kh ? m0 : m1, mk = kh ? m1 : m0;
        const unsigned xa = sb + L2_X + w * 2560, xb = sb + L2_X + (w ^ 4) * 2560;
        *(LDS3 f32x4*)(size_t)(xa + l16) = f32x4{os[0], os[1], os[2], os[3]};
        *(LDS3 f32x4*)(size_t)(xa + 1024 + l16) = f32x4{os[4], os[5], os[6], os[7]};
        *(LDS3 f32x2*)(size_t)(xa + 2048 + lane * 8) = f32x2{os[8], ms};
        WAITV(0); WAITL(); BAR();
        asm volatile("" :: "v"(warm));
        const f32x4 a = *(LDS3 f32x4*)(size_t)(xb + l16), c = *(LDS3 f32x4*)(size_t)(xb + 1024 + l16);
        const f32x2 lm = *(LDS3 f32x2*)(size_t)(xb + 2048 + lane * 8);
        const float mm = fmaxf(mk, lm[1]), ea = __builtin_amdgcn_exp2f(mk - mm), eb = __builtin_amdgcn_exp2f(lm[1] - mm);
        const float inv = 1.f / (ok[8] * ea + lm[0] * eb), fa = ea * inv, fb = eb * inv;
        u32x4 of;
        of[0] = cvtpk(ok[0] * fa + a[0] * fb, ok[1] * fa + a[1] * fb); of[1] = cvtpk(ok[2] * fa + a[2] * fb, ok[3] * fa + a[3] * fb);
        of[2] = cvtpk(ok[4] * fa + c[0] * fb, ok[5] * fa + c[1] * fb); of[3] = cvtpk(ok[6] * fa + c[2] * fb, ok[7] * fa + c[3] * fb);
        STC(clean(of), l16, O_OFF + ((g * 2 + kh) * 32 + 4 * j + (w & 3)) * 1024);
    }
    const unsigned xo = (64 * g + 32 * tt + r) * 512 + 64 * j + 32 * ct + 16 * kh;
    const u32x2 xa0 = *(const u32x2*)(x + xo + 4 * h), xa1 = *(const u32x2*)(x + xo + 8 + 4 * h);
    u32x2 bb[4];
#pragma unroll
    for (int i = 0; i < 4; i++) bb[i] = *(const u32x2*)(b1 + 224 * j + 32 * (w < 7 ? w : 6) + 8 * i + 4 * h);
    const unsigned myr = sb + kh * 65536 + 32768 + (w & 3) * 8192;
    const unsigned w2o = W2_OFF + (j * 224 + w * 28) * 1024;
    float hv[8];
    SB; COLSUM(0, 24) SB;
    {
        const int kq = w >> 1;
        u32x4 of[8], af[4];
        const unsigned wa = sb + kq * 8192 + l16;
        af[0] = lds128(wa); af[1] = lds128(wa + 65536); af[2] = lds128(wa + 1024); af[3] = lds128(wa + 65536 + 1024);
        poll8(of, rw, l16, O_OFF + ((g * 2 + tt) * 32 + 8 * kq) * 1024, 1024);
        u32x2 xb0 = xa0, xb1 = xa1;
        asm volatile("" : "+v"(xb0), "+v"(xb1), "+v"(bb[0]), "+v"(bb[1]), "+v"(bb[2]), "+v"(bb[3]));
        f32x16 acc[2] = {z16, z16};
#pragma unroll
        for (int i = 0; i < 16; i++) {
            acc[i & 1] = MF32(B8(af[i & 3]), B8(of[i >> 1]), acc[i & 1]); SB;
            if (i < 12) { af[i & 3] = lds128(wa + (i & 1) * 65536 + ((i >> 1) + 2) * 1024); SB; }
            if (i & 1) { w1f[24 + (i >> 1)] = LDA(w1o + (24 + (i >> 1)) * 1024); SB; }
        }
#pragma unroll
        for (int p = 0; p < 4; p++) {
            const unsigned xa = sb + (p >> 1) * 65536 + 32768 + (tt + 2 * (p & 1)) * 8192 + kq * 2048 + l16, o = (p >> 1) * 8;
            const f32x16 v = acc[p & 1];
            *(LDS3 f32x4*)(size_t)xa = f32x4{v[o], v[o + 1], v[o + 2], v[o + 3]};
            *(LDS3 f32x4*)(size_t)(xa + 1024) = f32x4{v[o + 4], v[o + 5], v[o + 6], v[o + 7]};
        }
        WAITL(); BAR();
        f32x4 a = {blo(xb0[0]), bhi(xb0[0]), blo(xb0[1]), bhi(xb0[1])}, c = {blo(xb1[0]), bhi(xb1[0]), blo(xb1[1]), bhi(xb1[1])};
#pragma unroll
        for (int p = 0; p < 4; p++) { a += *(LDS3 f32x4*)(size_t)(myr + p * 2048 + l16); c += *(LDS3 f32x4*)(size_t)(myr + p * 2048 + 1024 + l16); }
#pragma unroll
        for (int i = 0; i < 4; i++) { hv[i] = a[i]; hv[4 + i] = c[i]; }
        STC(clean(u32x4{cvtpk(hv[0], hv[1]), cvtpk(hv[2], hv[3]), cvtpk(hv[4], hv[5]), cvtpk(hv[6], hv[7])}), l16, hslot);
    }
    SB; COLSUM(24, 32) SB;
#pragma unroll
    for (int i = 0; i < 4; i++) dma16(rw, l16, w2o + i * 1024, i < 8 ? myr + i * 1024 : sb + L2_W2 + (w * 3 + i - 8) * 1024);
    {
        u32x4 hf[8];
        poll8(hf, rw, l16, H_OFF + (g * 64 + 8 * w) * 1024, 1024);
#pragma unroll
        for (int i = 0; i < 8; i++) sts128(sb + (i >> 2) * 65536 + (4 * w + (i & 3)) * 1024 + l16, hf[i]);
        f32x4 st;
#pragma unroll
        for (int t = 0; t < 2; t++) {
            float s0 = 0.f, q0_ = 0.f, s1 = 0.f, q1_ = 0.f;
#pragma unroll
            for (int i = 0; i < 4; i++)
#pragma unroll
                for (int e = 0; e < 4; e += 2) {
                    s0 = dot2(hf[4 * t + i][e], 0x3f803f80u, s0); q0_ = dot2(hf[4 * t + i][e], hf[4 * t + i][e], q0_);
                    s1 = dot2(hf[4 * t + i][e + 1], 0x3f803f80u, s1); q1_ = dot2(hf[4 * t + i][e + 1], hf[4 * t + i][e + 1], q1_);
                }
            st[2 * t] = xsum32(s0 + s1); st[2 * t + 1] = xsum32(q0_ + q1_);
        }
        if (h == 0) *(LDS3 f32x4*)(size_t)(sb + L2_STAT + w * 512 + r * 16) = st;
        if (tid == 0) *(LDS3 unsigned*)(size_t)(sb + L2_ONES) = 0;
        WAITL(); BAR();
    }
    __builtin_amdgcn_s_setprio(1);
    u32x4 w2f[17], fa[7], fb[7];
    const unsigned ab = A_OFF + (g * 8 + w) * 28 * 1024;
    {
        f32x16 ac[2] = {z16, z16};
        u32x4 bf[4];
        unsigned ha0 = sb + l16, ha1 = sb + 65536 + l16;
        asm volatile("" : "+v"(ha1));
#pragma unroll
        for (int i = 0; i < 4; i++) bf[i] = lds128(((i & 1) ? ha1 : ha0) + (i >> 1) * 1024);
        f32x4 st = {0, 0, 0, 0};
        float mean[2], rstd[2];
        SB;
        sfor<64>([&](auto K) __attribute__((always_inline)) {
            constexpr int n = K.v;
            ac[n & 1] = MF32(B8(w1f[n >> 1]), B8(bf[n & 3]), ac[n & 1]); SB;
            if (n < 60) { bf[n & 3] = lds128(((n & 1) ? ha1 : ha0) + ((n >> 1) + 2) * 1024); SB; }
            if (n >= 4 && n < 12) { st += *(LDS3 f32x4*)(size_t)(sb + L2_STAT + (n - 4) * 512 + r * 16); SB; }
            if (n == 16) {
#pragma unroll
                for (int t = 0; t < 2; t++) {
                    mean[t] = st[2 * t] * (1.f / 512.f);
                    rstd[t] = __builtin_amdgcn_rsqf(fmaxf(st[2 * t + 1] * (1.f / 512.f) - mean[t] * mean[t], 0.f) + 1e-5f);
                }
                SB;
            }
            constexpr int m = (n >> 1) + 4;
            if ((n & 1) && m < 11) { dma16(rw, l16, w2o + m * 1024, m < 8 ? myr + m * 1024 : sb + L2_W2 + (w * 3 + m - 8) * 1024); SB; }
            if ((n & 1) && m >= 11 && m < 28) { w2f[m >= 11 && m < 28 ? m - 11 : 0] = LDW(l16, w2o + m * 1024); SB; }
        });
#define PIN(x) asm volatile("" : "+v"(x));
        sfor<4>([&](auto K) __attribute__((always_inline)) {
            constexpr int t = K.v >> 1, s = K.v & 1;
            float z[8], u[8];
            u32x4 yp;
#define EL8 _Pragma("unroll") for (int i = 0; i < 8; i++)
            EL8 { u[i] = fmaf(-mean[t], cs[8 * s + i], ac[t][8 * s + i]); PIN(u[i]) }
            EL8 { const unsigned bw = bb[2 * s + (i >> 2)][(i >> 1) & 1]; z[i] = (i & 1) ? bhi(bw) : blo(bw); PIN(z[i]) }
            EL8 { z[i] = fmaf(u[i], rstd[t], z[i]); PIN(z[i]) }
            EL8 { u[i] = z[i] * LOG2E; PIN(u[i]) }
            EL8 { u[i] = __builtin_amdgcn_exp2f(u[i]); PIN(u[i]) }
            float a_[8];
            EL8 { a_[i] = u[i] + 2.f; PIN(a_[i]) }
            EL8 { u[i] = fmaf(u[i], a_[i], 2.f); PIN(u[i]) }
            EL8 { u[i] = __builtin_amdgcn_rcpf(u[i]); PIN(u[i]) }
            EL8 { u[i] = fmaf(u[i], -2.f, 1.f); PIN(u[i]) }
            EL8 { z[i] *= u[i]; PIN(z[i]) }
            EL8 if (i < 4) { yp[i & 3] = cvtpk(z[2 * (i & 3)], z[2 * (i & 3) + 1]); PIN(yp[i & 3]) }
            STA(clean(yp), aslot + (t * 14 + s) * 1024); SB;
            if (K.v == 1) {
                asm volatile("" : "+v"(w2f[0]), "+v"(w2f[1]), "+v"(w2f[2]), "+v"(w2f[3]), "+v"(w2f[4]), "+v"(w2f[5]), "+v"(w2f[6]), "+v"(w2f[7]), "+v"(w2f[8]),
                                  "+v"(w2f[9]), "+v"(w2f[10]), "+v"(w2f[11]), "+v"(w2f[12]), "+v"(w2f[13]), "+v"(w2f[14]), "+v"(w2f[15]), "+v"(w2f[16]));
#pragma unroll
                for (int i = 0; i < 7; i++) asm volatile("" : "=v"(fa[i]), "=v"(fb[i]));
                issue7if(fa, kh, rw, l16, ab); issue7if(fb, kh, rw, l16, ab + 14336); SB;
            }
        });
    }
    {
        u32x4 wl[2];
        __builtin_amdgcn_s_setprio(0);
        LDS3 unsigned* done = (LDS3 unsigned*)(size_t)(sb + L2_ONES);
        if (lane == 0) __hip_atomic_fetch_add(done, 1u, __ATOMIC_RELAXED, __HIP_MEMORY_SCOPE_WORKGROUP);
#define ALLDONE while (__builtin_amdgcn_readfirstlane(__hip_atomic_load(done, __ATOMIC_RELAXED, __HIP_MEMORY_SCOPE_WORKGROUP)) < 8) __builtin_amdgcn_s_sleep(2);
        const unsigned pv = (lane < 7 ? lane : lane < 14 ? lane + 7 : 0) * 1024;
        issue7if(fa, kh ^ 1, rw, l16, ab);
        if (!kh) probe(rw, min(l16, 96u) * 64, ab + 14336);
        issue7if(fb, kh ^ 1, rw, l16, ab + 14336);
        unsigned wla = myr + l16, wlb = sb + L2_W2 + w * 3072 + l16;
        asm volatile("" : "+v"(wla), "+v"(wlb));
#define WLD(n) lds128((n) < 8 ? wla + (n) * 1024 : wlb + ((n) - 8) * 1024)
#define F2(F, ks0, C0, C1) _Pragma("unroll") for (int k = 0; k < 7; k++) { const int n0 = 2 * ((ks0) + k), n1 = n0 + 1;               \
            C0 = MF32(B8(n0 < 11 ? wl[0] : w2f[n0 < 11 ? 0 : n0 - 11]), B8(F[k]), C0); SB; if (n0 + 2 < 11) { wl[0] = WLD(n0 + 2); SB; } \
            C1 = MF32(B8(n1 < 11 ? wl[1] : w2f[n1 < 11 ? 0 : n1 - 11]), B8(F[k]), C1); SB; if (n1 + 2 < 11) { wl[1] = WLD(n1 + 2); SB; } }
#define PST(v, t, cc, s) sts128(sb + (t) * 65536 + ((cc) + 2 * (s)) * 8192 + w * 1024 + l16,                                          \
            u32x4{cvtpk(v[8 * (s)], v[8 * (s) + 1]), cvtpk(v[8 * (s) + 2], v[8 * (s) + 3]), cvtpk(v[8 * (s) + 4], v[8 * (s) + 5]), cvtpk(v[8 * (s) + 6], v[8 * (s) + 7])});
        f32x16 c0 = z16, c1 = z16, c2 = z16, c3 = z16;
        wl[0] = WLD(0); wl[1] = WLD(1);
        check7<7>(fa, rw, l16, ab);
        F2(fa, 0, c0, c1)
        issue7if(fa, kh, rw, l16, ab + 7168);
        wl[0] = WLD(0); wl[1] = WLD(1);
        if (!kh) WAITV(0);
        check7<7>(fb, rw, l16, ab + 14336);
        F2(fb, 0, c2, c3)
        if (!kh) { ALLDONE probe(rw, pv, ab + 7168); }
        issue7if(fa, kh ^ 1, rw, l16, ab + 7168);
        issue7(fb, rw, l16, ab + 21504);
        check7<7>(fa, rw, l16, ab + 7168);
        F2(fa, 7, c0, c1)
        if (kh) { ALLDONE asm volatile("" ::: "memory"); }
        PST(c0, 0, 0, 0) PST(c0, 0, 0, 1) PST(c1, 0, 1, 0) PST(c1, 0, 1, 1)
        check7<0>(fb, rw, l16, ab + 21504);
        F2(fb, 7, c2, c3)
        PST(c2, 1, 0, 0) PST(c2, 1, 0, 1) PST(c3, 1, 1, 0) PST(c3, 1, 1, 1)
        WAITL(); BAR();
        unsigned sl = 0x00003f80u, sh = 0x3f800000u;
        asm volatile("" : "+s"(sl), "+s"(sh));
#pragma unroll
        for (int i = 0; i < 8; i++) {
            const u32x4 pf = lds128(sb + tt * 65536 + (w >> 1) * 8192 + i * 1024 + l16);
#pragma unroll
            for (int e = 0; e < 4; e++) { hv[2 * e] = dot2(pf[e], sl, hv[2 * e]); hv[2 * e + 1] = dot2(pf[e], sh, hv[2 * e + 1]); }
        }
        const u32x2 s0 = __builtin_amdgcn_permlane32_swap(cvtpk(hv[0], hv[1]), cvtpk(hv[4], hv[5]), false, false);
        const u32x2 s1 = __builtin_amdgcn_permlane32_swap(cvtpk(hv[2], hv[3]), cvtpk(hv[6], hv[7]), false, false);
        *(u32x4*)(out + xo + 8 * h) = u32x4{s0[0], s1[0], s0[1], s1[1]};
    }
}

extern "C" void kernel_launch(void* const* d_in, const int* in_sizes, int n_in, void* d_out, int out_size, void* d_ws, size_t ws_size,
                              hipStream_t stream) {
    typedef const unsigned short* P;
    k1_kernel<<<256, 512, 0, stream>>>((P)d_in[0], (P)d_in[1], (P)d_in[2], (P)d_in[3], (P)d_in[4], (P)d_in[6], (P)d_in[8], (char*)d_ws);
    k2_kernel<<<256, 512, 0, stream>>>((P)d_in[0], (P)d_in[7], (unsigned short*)d_out, (char*)d_ws);
}
```
